```python
import torch

def _softmax(x, axis=-1):
    return torch.softmax(x, dim=axis)
def _gelu(x):
    return torch.nn.functional.gelu(x, approximate="tanh")
def _silu(x):
    return torch.nn.functional.silu(x)
def _sigmoid(x):
    return torch.sigmoid(x)
def _layernorm(x, axis=-1, eps=1e-5):
    m = x.mean(dim=axis, keepdim=True)
    v = x.var(dim=axis, keepdim=True, unbiased=False)
    return (x - m) / torch.sqrt(v + eps)
def _scatter_add(tgt, idx, src, axis=0):
    return tgt.clone().index_add_(axis, idx, src)
def _segment_sum(src, seg, n):
    out = torch.zeros((n,) + src.shape[1:], dtype=src.dtype)
    return out.index_add_(0, seg, src)
def _pad(x, pw):
    flat = tuple(v for pair in reversed(pw) for v in pair)
    return torch.nn.functional.pad(x, flat)

kernel_name = 'concat_heads__mha_block_cached__mha_block_cached__near_ridge'

def setup_inputs():

    _w = (lambda a: (lambda t: t.to(torch.bfloat16) if t.is_floating_point() else t)(torch.as_tensor(a)))
    def _A_setup():
        B, Sq, D = 8, 128, 576
        H, Sk, Dh = 32, 1024, 128
        return {
            "x": _w((lambda *s: torch.randn(*s, dtype=torch.bfloat16) * 0.8)(B, Sq, D)),
            "k_cache": _w((lambda *s: torch.randn(*s, dtype=torch.bfloat16) * 0.8)(B, H, Sk, Dh)),
            "v_cache": _w((lambda *s: torch.randn(*s, dtype=torch.bfloat16) * 0.05)(B, H, Sk, Dh)),
            "wq": _w((lambda *s: torch.randn(*s, dtype=torch.bfloat16) * 0.8)(D, H * Dh) * (D ** -0.5)),
            "wo": _w((lambda *s: torch.randn(*s, dtype=torch.bfloat16) * 0.05)(H * Dh, D) * ((H * Dh) ** -0.5)),
        }
    _A_in = _A_setup()

    def _B_setup():
        B, Sq, D = 8, 128, 576
        H, Sk, Dh = 32, 1024, 128
        return {
            "x": _w((lambda *s: torch.randn(*s, dtype=torch.bfloat16) * 0.8)(B, Sq, D)),
            "k_cache": _w((lambda *s: torch.randn(*s, dtype=torch.bfloat16) * 0.8)(B, H, Sk, Dh)),
            "v_cache": _w((lambda *s: torch.randn(*s, dtype=torch.bfloat16) * 0.05)(B, H, Sk, Dh)),
            "wq": _w((lambda *s: torch.randn(*s, dtype=torch.bfloat16) * 0.8)(D, H * Dh) * (D ** -0.5)),
            "wo": _w((lambda *s: torch.randn(*s, dtype=torch.bfloat16) * 0.05)(H * Dh, D) * ((H * Dh) ** -0.5)),
        }
    _B_in = _B_setup()

    return {
        "A_x": _A_in["x"],
        "A_k_cache": _A_in["k_cache"],
        "A_v_cache": _A_in["v_cache"],
        "A_wq": _A_in["wq"],
        "A_wo": _A_in["wo"],
        "B_x": _B_in["x"],
        "B_k_cache": _B_in["k_cache"],
        "B_v_cache": _B_in["v_cache"],
        "B_wq": _B_in["wq"],
        "B_wo": _B_in["wo"],
    }

def reference(A_x, A_k_cache, A_v_cache, A_wq, A_wo, B_x, B_k_cache, B_v_cache, B_wq, B_wo):
    def _A_ref(x, k_cache, v_cache, wq, wo):
        B, Sq, D = x.shape
        H, Dh = 32, 128
        q = (lambda x, *d: x.permute(*d))((x @ wq).reshape(B, Sq, H, Dh), 0, 2, 1, 3)
        scores = torch.einsum("bhqd,bhkd->bhqk", q, k_cache) * (Dh ** -0.5)
        p = _softmax(scores, axis=-1)
        o = torch.einsum("bhqk,bhkd->bhqd", p, v_cache)
        return (lambda x, *d: x.permute(*d))(o, 0, 2, 1, 3).reshape(B, Sq, H * Dh) @ wo

    def _B_ref(x, k_cache, v_cache, wq, wo):
        B, Sq, D = x.shape
        H, Dh = 32, 128
        q = (lambda x, *d: x.permute(*d))((x @ wq).reshape(B, Sq, H, Dh), 0, 2, 1, 3)
        scores = torch.einsum("bhqd,bhkd->bhqk", q, k_cache) * (Dh ** -0.5)
        p = _softmax(scores, axis=-1)
        o = torch.einsum("bhqk,bhkd->bhqd", p, v_cache)
        return (lambda x, *d: x.permute(*d))(o, 0, 2, 1, 3).reshape(B, Sq, H * Dh) @ wo

    a = _A_ref(A_x, A_k_cache, A_v_cache, A_wq, A_wo)
    b = _B_ref(B_x, B_k_cache, B_v_cache, B_wq, B_wo)
    return torch.concatenate([a, b], axis=-1)
```

```cpp
#include <hip/hip_runtime.h>
#include <hip/hip_bf16.h>
#include <cstdint>

typedef short bf16x8 __attribute__((ext_vector_type(8)));
typedef short s16x4 __attribute__((ext_vector_type(4)));
typedef float f32x16 __attribute__((ext_vector_type(16)));
typedef float f32x4 __attribute__((ext_vector_type(4)));
typedef float f32x2 __attribute__((ext_vector_type(2)));
typedef __bf16 bf16x2_t __attribute__((ext_vector_type(2)));
typedef unsigned u32x4 __attribute__((ext_vector_type(4)));
typedef unsigned u32x2 __attribute__((ext_vector_type(2)));
#define LAS __attribute__((address_space(3)))
#define DI __device__ __forceinline__
typedef LAS const char* lds_cptr;
typedef LAS char* lds_ptr;

constexpr int SLOT = 16384;
constexpr int LDS_BYTES = 163840;
constexpr float CSC = 0.08838834764831845f * 1.4426950408889634f;
constexpr float THR = 8.0f;

#define MFMA32(a, b, c) __builtin_amdgcn_mfma_f32_32x32x16_bf16((a), (b), (c), 0, 0, 0)
#define MFMA16(a, b, c) __builtin_amdgcn_mfma_f32_16x16x32_bf16((a), (b), (c), 0, 0, 0)

DI unsigned cvtpk(float lo, float hi) { f32x2 v = {lo, hi}; bf16x2_t b = __builtin_convertvector(v, bf16x2_t); return __builtin_bit_cast(unsigned, b); }
DI s16x4 vtr(lds_cptr p) { return __builtin_bit_cast(s16x4, __builtin_amdgcn_ds_read_tr16_b64_v4i16((LAS s16x4*)p)); }
DI bf16x8 cat8(s16x4 a, s16x4 b) { return __builtin_shufflevector(a, b, 0, 1, 2, 3, 4, 5, 6, 7); }
DI bf16x8 ldsrd128(lds_cptr p) { return *(const LAS bf16x8*)p; }
DI float swap32max(float m) { auto rr = __builtin_amdgcn_permlane32_swap(__float_as_uint(m), __float_as_uint(m), false, false); return __builtin_fmaxf(__uint_as_float(rr[0]), __uint_as_float(rr[1])); }
DI float swap32sum(float m) { auto rr = __builtin_amdgcn_permlane32_swap(__float_as_uint(m), __float_as_uint(m), false, false); return __uint_as_float(rr[0]) + __uint_as_float(rr[1]); }

DI void dma16_nt(unsigned ldsdst, unsigned voff, __amdgpu_buffer_rsrc_t srd, unsigned soff) {
    unsigned sv;
    asm volatile("s_mov_b32 %0, m0\n\ts_mov_b32 m0, %1\n\ts_nop 0\n\tbuffer_load_dwordx4 %2, %3, %4 offen nt lds\n\ts_mov_b32 m0, %0"
                 : "=&s"(sv) : "s"(ldsdst), "v"(voff), "s"(srd), "s"(soff) : "memory");
}
DI void dma16(unsigned ldsdst, unsigned voff, __amdgpu_buffer_rsrc_t srd, unsigned soff) {
    unsigned sv;
    asm volatile("s_mov_b32 %0, m0\n\ts_mov_b32 m0, %1\n\ts_nop 0\n\tbuffer_load_dwordx4 %2, %3, %4 offen lds\n\ts_mov_b32 m0, %0"
                 : "=&s"(sv) : "s"(ldsdst), "v"(voff), "s"(srd), "s"(soff) : "memory");
}
#define RFL(x) __builtin_amdgcn_readfirstlane(x)

template <int OFF> DI void xload(bf16x8& dst, unsigned voff, __amdgpu_buffer_rsrc_t srd) {
    asm volatile("buffer_load_dwordx4 %0, %1, %2, 0 offen offset:%3" : "=v"(dst) : "v"(voff), "s"(srd), "i"(OFF) : "memory");
}

__global__ __launch_bounds__(512, 2) void attn_kernel(const char* __restrict__ xA, const char* __restrict__ kA, const char* __restrict__ vA, const char* __restrict__ wqA,
                                                     const char* __restrict__ xB, const char* __restrict__ kB, const char* __restrict__ vB, const char* __restrict__ wqB,
                                                     char* __restrict__ Ows) {
    extern __shared__ __attribute__((aligned(16))) char lds[];
    const int tid = threadIdx.x, lane = tid & 63;
    const int w = RFL(tid >> 6);
    const int r32 = lane & 31, hi = lane >> 5;
    const int wg = blockIdx.x, xcd = wg & 7, idx = wg >> 3;
    const int p = xcd & 1, h = 8 * (xcd >> 1) + (idx & 7), bq = idx >> 3;
    const unsigned lds0 = (unsigned)(uintptr_t)lds;
    const lds_cptr L = (lds_cptr)lds;
    const __amdgpu_buffer_rsrc_t srdX = __builtin_amdgcn_make_buffer_rsrc((void*)(p ? xB : xA), (short)0, 8 * 128 * 576 * 2, 0x00020000);
    const __amdgpu_buffer_rsrc_t srdW = __builtin_amdgcn_make_buffer_rsrc((void*)(p ? wqB : wqA), (short)0, 576 * 4096 * 2, 0x00020000);
    const __amdgpu_buffer_rsrc_t srdK = __builtin_amdgcn_make_buffer_rsrc((void*)(p ? kB : kA), (short)0, 8 * 32 * 1024 * 256, 0x00020000);
    const __amdgpu_buffer_rsrc_t srdV = __builtin_amdgcn_make_buffer_rsrc((void*)(p ? vB : vA), (short)0, 8 * 32 * 1024 * 256, 0x00020000);

    const int r7 = (lane >> 2) & 7;
    const unsigned chsw = 16u * (4u * (unsigned)hi + (unsigned)((lane & 3) ^ ((2 * w + (r7 >> 2)) & 3)));
    const unsigned srcKV = (unsigned)(8 * w + r7) * 256u + chsw;
    const unsigned srcW = (unsigned)(8 * w + r7) * 8192u + chsw;
    const unsigned dstw = lds0 + 2048u * (unsigned)w;

    auto kv_off = [&](int T) -> unsigned { const int r = T >> 4, t = T & 15; return (unsigned)(((bq + 4 * r) * 32 + h) * 1024 + 64 * t) * 256u; };
    auto dma_k = [&](int T) { const unsigned so = kv_off(T), d = dstw + (unsigned)((2 * T) & 7) * SLOT;
        dma16_nt(RFL(d), srcKV, srdK, RFL(so)); dma16_nt(RFL(d + 1024), srcKV, srdK, RFL(so + 128)); };
    auto dma_v = [&](int T) { const unsigned so = kv_off(T), d = dstw + (unsigned)((2 * T + 1) & 7) * SLOT;
        dma16_nt(RFL(d), srcKV, srdV, RFL(so)); dma16_nt(RFL(d + 1024), srcKV, srdV, RFL(so + 128)); };

    bf16x8 xf[36];
    const unsigned xoff = (unsigned)(((bq + 4 * (w >> 2)) * 128 + 32 * (w & 3) + r32) * 576 + 8 * hi) * 2u;
#define XL(i) xload<32 * (i)>(xf[i], xoff, srdX)
#define WCH(c) do { const unsigned so_ = (unsigned)(64 * (c)) * 8192u + (unsigned)h * 256u, d_ = dstw + (unsigned)((c) + 1) * SLOT; \
        dma16(RFL(d_), srcW, srdW, RFL(so_)); dma16(RFL(d_ + 1024), srcW, srdW, RFL(so_ + 128)); } while (0)
#define ISS(c) do { XL(4 * (c)); XL(4 * (c) + 1); XL(4 * (c) + 2); XL(4 * (c) + 3); WCH(c); } while (0)
    ISS(0); ISS(1); ISS(2); ISS(3); ISS(4); ISS(5); ISS(6); ISS(7); ISS(8);
    dma_k(0);

    const int blk = (lane >> 4) & 1, q4 = (lane & 15) >> 2, p4 = lane & 3;
    const unsigned wb0 = 2048u * hi + 64u * (0 + q4) + 16u * ((2 * blk + (p4 & 1)) ^ (2 * hi + 0)) + 8u * (p4 >> 1);
    const unsigned wb1 = 2048u * hi + 64u * (4 + q4) + 16u * ((2 * blk + (p4 & 1)) ^ (2 * hi + 1)) + 8u * (p4 >> 1);

    f32x16 acc[4];
#pragma unroll
    for (int i = 0; i < 4; ++i) acc[i] = f32x16{};
#define QSTEP(c, N) do { \
        asm volatile("s_waitcnt vmcnt(" #N ") lgkmcnt(0)\n\ts_barrier" : "+v"(xf[4 * (c)]), "+v"(xf[4 * (c) + 1]), "+v"(xf[4 * (c) + 2]), "+v"(xf[4 * (c) + 3]) :: "memory"); \
        if ((c) >= 1 && (c) <= 7) { if (((c) & 1) == 0) dma_k((c) >> 1); else dma_v((c) >> 1); } \
        const lds_cptr cb_ = L + ((c) + 1) * SLOT; \
        _Pragma("unroll") for (int ks = 0; ks < 4; ++ks) { _Pragma("unroll") for (int db = 0; db < 4; ++db) { \
            const bf16x8 a_ = cat8(vtr(cb_ + wb0 + 4096 * ks + 512 * db), vtr(cb_ + wb1 + 4096 * ks + 512 * db)); \
            acc[db] = MFMA32(a_, xf[4 * (c) + ks], acc[db]); } } \
    } while (0)
    QSTEP(0, 50); QSTEP(1, 44); QSTEP(2, 40); QSTEP(3, 36); QSTEP(4, 32); QSTEP(5, 28); QSTEP(6, 24); QSTEP(7, 20); QSTEP(8, 16);

    bf16x8 qf[8];
#pragma unroll
    for (int db = 0; db < 4; ++db)
#pragma unroll
        for (int sg = 0; sg < 2; ++sg) {
            u32x4 wv = {cvtpk(acc[db][8 * sg + 0], acc[db][8 * sg + 1]), cvtpk(acc[db][8 * sg + 2], acc[db][8 * sg + 3]), cvtpk(acc[db][8 * sg + 4], acc[db][8 * sg + 5]), cvtpk(acc[db][8 * sg + 6], acc[db][8 * sg + 7])};
            qf[2 * db + sg] = __builtin_bit_cast(bf16x8, wv);
        }
    const int qb = w & 3, kh = w >> 2;
    const lds_ptr misc = (lds_ptr)lds + 8 * SLOT;
    const unsigned qpark = (unsigned)(qb * 8) * 1024u + (unsigned)lane * 16u;
    asm volatile("s_waitcnt lgkmcnt(0)\n\ts_barrier" ::: "memory");
    if (kh == 0) {
#pragma unroll
        for (int s = 0; s < 8; ++s) *(LAS bf16x8*)(misc + qpark + 1024 * s) = qf[s];
    }
    asm volatile("s_waitcnt lgkmcnt(0)\n\ts_barrier" ::: "memory");
    bf16x8 qt[8];
    if (kh == 1) {
#pragma unroll
        for (int s = 0; s < 8; ++s) qt[s] = *(const LAS bf16x8*)(misc + qpark + 1024 * s);
    }
    asm volatile("s_waitcnt lgkmcnt(0)\n\ts_barrier" ::: "memory");
    if (kh == 1) {
#pragma unroll
        for (int s = 0; s < 8; ++s) { *(LAS bf16x8*)(misc + qpark + 1024 * s) = qf[s]; qf[s] = qt[s]; }
    }

    const int krow = 32 * kh + r32;
    const unsigned kb0 = 2048u * (krow >> 3) + 64u * (krow & 7) + 16u * ((0 + hi) ^ ((krow >> 2) & 3));
    const unsigned kb1 = 2048u * (krow >> 3) + 64u * (krow & 7) + 16u * ((2 + hi) ^ ((krow >> 2) & 3));
    const unsigned vb0 = 2048u * (4 * kh + 0) + 64u * (4 * hi + q4) + 16u * ((2 * blk + (p4 >> 1)) ^ (0 + hi)) + 8u * (p4 & 1);
    const unsigned vb1 = 2048u * (4 * kh + 1) + 64u * (4 * hi + q4) + 16u * ((2 * blk + (p4 >> 1)) ^ (2 + hi)) + 8u * (p4 & 1);

    f32x16 o[4];
#pragma unroll
    for (int i = 0; i < 4; ++i) o[i] = f32x16{};
    float mref = -INFINITY, lsum = 0.f;

    for (int T = 0; T < 32; ++T) {
        if (T == 0 || (T >= 16 && T <= 18)) asm volatile("s_waitcnt vmcnt(12) lgkmcnt(0)\n\ts_barrier" ::: "memory");
        else if (T < 30) asm volatile("s_waitcnt vmcnt(8) lgkmcnt(0)\n\ts_barrier" ::: "memory");
        else if (T == 30) asm volatile("s_waitcnt vmcnt(4) lgkmcnt(0)\n\ts_barrier" ::: "memory");
        else asm volatile("s_waitcnt vmcnt(0) lgkmcnt(0)\n\ts_barrier" ::: "memory");
        if (T >= 1 && T + 3 < 32) { dma_k(T + 3); dma_v(T + 3); }
        const lds_cptr ks_ = L + ((2 * T) & 7) * SLOT, vs_ = L + ((2 * T + 1) & 7) * SLOT;
        f32x16 s = f32x16{};
#pragma unroll
        for (int ks = 0; ks < 8; ++ks) {
            const bf16x8 kf = ldsrd128(ks_ + ((ks & 1) ? kb1 : kb0) + 512 * (ks >> 1));
            s = MFMA32(kf, qf[ks], s);
        }
        float mx = __builtin_fmaxf(s[0], s[1]);
#pragma unroll
        for (int i = 2; i < 16; i += 2) mx = __builtin_fmaxf(__builtin_fmaxf(mx, s[i]), s[i + 1]);
        mx = swap32max(mx) * CSC;
        if (__builtin_expect(__any(mx - mref > THR), 0)) {
            const float mn = __builtin_fmaxf(mref, mx); const float al = __builtin_amdgcn_exp2f(mref - mn); mref = mn; lsum *= al;
#pragma unroll
            for (int d = 0; d < 4; ++d)
#pragma unroll
                for (int i = 0; i < 16; ++i) o[d][i] *= al;
        }
        const float nm = -mref;
#pragma unroll
        for (int i = 0; i < 16; ++i) s[i] = __builtin_amdgcn_exp2f(__builtin_fmaf(s[i], CSC, nm));
        float ps = 0.f;
#pragma unroll
        for (int i = 0; i < 16; ++i) ps += s[i];
        lsum += ps;
        bf16x8 pk[2];
#pragma unroll
        for (int sg = 0; sg < 2; ++sg) {
            u32x4 wv = {cvtpk(s[8 * sg + 0], s[8 * sg + 1]), cvtpk(s[8 * sg + 2], s[8 * sg + 3]), cvtpk(s[8 * sg + 4], s[8 * sg + 5]), cvtpk(s[8 * sg + 6], s[8 * sg + 7])};
            pk[sg] = __builtin_bit_cast(bf16x8, wv);
        }
#pragma unroll
        for (int sg = 0; sg < 2; ++sg)
#pragma unroll
            for (int db = 0; db < 4; ++db) {
                const bf16x8 vf = cat8(vtr(vs_ + vb0 + 4096 * sg + 512 * db), vtr(vs_ + vb1 + 4096 * sg + 512 * db));
                o[db] = MFMA32(vf, pk[sg], o[db]);
            }
        if ((T & 15) == 15) {
            const int r = T >> 4;
            if (r == 0) {
#pragma unroll
                for (int s2 = 0; s2 < 8; ++s2) qf[s2] = *(const LAS bf16x8*)(misc + qpark + 1024 * s2);
            }
            const float lt = swap32sum(lsum);
            asm volatile("s_waitcnt lgkmcnt(0)\n\ts_barrier" ::: "memory");
            if (hi == 0) *(LAS f32x2*)(misc + (unsigned)(kh * 128 + qb * 32 + r32) * 8u) = f32x2{mref, lt};
            asm volatile("s_waitcnt lgkmcnt(0)\n\ts_barrier" ::: "memory");
            const f32x2 ot = *(const LAS f32x2*)(misc + (unsigned)((kh ^ 1) * 128 + qb * 32 + r32) * 8u);
            const float M = __builtin_fmaxf(mref, ot[0]);
            const float f0 = __builtin_amdgcn_exp2f(mref - M), f1 = __builtin_amdgcn_exp2f(ot[0] - M);
            const float sc = f0 / (lt * f0 + ot[1] * f1);
            asm volatile("s_waitcnt lgkmcnt(0)\n\ts_barrier" ::: "memory");
            f32x16 keep0 = kh ? o[2] : o[0], keep1 = kh ? o[3] : o[1], send0 = kh ? o[0] : o[2], send1 = kh ? o[1] : o[3];
            const unsigned xw = (unsigned)w * 4096u + (unsigned)lane * 16u, xr = (unsigned)(w ^ 4) * 4096u + (unsigned)lane * 16u;
#pragma unroll
            for (int g = 0; g < 4; ++g) *(LAS f32x4*)(misc + xw + 1024 * g) = f32x4{send0[4 * g] * sc, send0[4 * g + 1] * sc, send0[4 * g + 2] * sc, send0[4 * g + 3] * sc};
            asm volatile("s_waitcnt lgkmcnt(0)\n\ts_barrier" ::: "memory");
#pragma unroll
            for (int g = 0; g < 4; ++g) { const f32x4 t4 = *(const LAS f32x4*)(misc + xr + 1024 * g);
#pragma unroll
                for (int e = 0; e < 4; ++e) keep0[4 * g + e] = keep0[4 * g + e] * sc + t4[e]; }
            asm volatile("s_waitcnt lgkmcnt(0)\n\ts_barrier" ::: "memory");
#pragma unroll
            for (int g = 0; g < 4; ++g) *(LAS f32x4*)(misc + xw + 1024 * g) = f32x4{send1[4 * g] * sc, send1[4 * g + 1] * sc, send1[4 * g + 2] * sc, send1[4 * g + 3] * sc};
            asm volatile("s_waitcnt lgkmcnt(0)\n\ts_barrier" ::: "memory");
#pragma unroll
            for (int g = 0; g < 4; ++g) { const f32x4 t4 = *(const LAS f32x4*)(misc + xr + 1024 * g);
#pragma unroll
                for (int e = 0; e < 4; ++e) keep1[4 * g + e] = keep1[4 * g + e] * sc + t4[e]; }
            asm volatile("s_waitcnt lgkmcnt(0)\n\ts_barrier" ::: "memory");
            const int qrow = 32 * qb + r32;
#pragma unroll
            for (int j = 0; j < 2; ++j) {
                const f32x16& kk = j ? keep1 : keep0; const int db = 2 * kh + j;
#pragma unroll
                for (int g = 0; g < 4; ++g) {
                    const u32x2 pv = {cvtpk(kk[4 * g], kk[4 * g + 1]), cvtpk(kk[4 * g + 2], kk[4 * g + 3])};
                    const int ch = 4 * db + g;
                    *(LAS u32x2*)(misc + (unsigned)qrow * 256u + 16u * (unsigned)(ch ^ (qrow & 15)) + 8u * hi) = pv;
                }
            }
            asm volatile("s_waitcnt lgkmcnt(0)\n\ts_barrier" ::: "memory");
            char* Ob = Ows + ((size_t)(p * 1024 + (bq + 4 * r) * 128) * 4096 + (size_t)h * 128) * 2;
#pragma unroll
            for (int i = 0; i < 4; ++i) {
                const int row = 16 * w + 4 * i + (lane >> 4), ch = lane & 15;
                const u32x4 v4 = *(const LAS u32x4*)(misc + (unsigned)row * 256u + 16u * (unsigned)(ch ^ (row & 15)));
                *(u32x4*)(Ob + (size_t)row * 8192 + 16 * ch) = v4;
            }
#pragma unroll
            for (int i = 0; i < 4; ++i) o[i] = f32x16{};
            mref = -INFINITY; lsum = 0.f;
        }
    }
}

__global__ __launch_bounds__(512, 2) void oproj_kernel(const char* __restrict__ Ows, const char* __restrict__ woA, const char* __restrict__ woB, float* __restrict__ slabs) {
    extern __shared__ __attribute__((aligned(16))) char lds[];
    const int tid = threadIdx.x, lane = tid & 63;
    const int w = RFL(tid >> 6);
    const int u = blockIdx.x;
    const int s8 = u & 7, c4 = (u >> 3) & 3, b = (u >> 5) & 7, p = u >> 8;
    const unsigned lds0 = (unsigned)(uintptr_t)lds;
    const lds_cptr L = (lds_cptr)lds;
    const __amdgpu_buffer_rsrc_t srdW = __builtin_amdgcn_make_buffer_rsrc((void*)(p ? woB : woA), (short)0, 4096 * 576 * 2, 0x00020000);
    const __amdgpu_buffer_rsrc_t srdO = __builtin_amdgcn_make_buffer_rsrc((void*)Ows, (short)0, 2048 * 4096 * 2, 0x00020000);
#pragma unroll
    for (int i = 0; i < 18; ++i) {
        const int uu = 64 * i + lane, pos = uu / 18, ch = uu - 18 * pos;
        const int k = (pos & ~12) | ((pos & 4) << 1) | ((pos & 8) >> 1);
        const unsigned vo = (unsigned)k * 1152u + 16u * ch;
        dma16(RFL(lds0 + (unsigned)w * 18432u + 1024u * i), vo, srdW, RFL((unsigned)(512 * s8 + 64 * w) * 1152u + 288u * c4));
    }
    const int mg = w & 3, kq = w >> 2, m16 = lane & 15, g4 = lane >> 4;
    bf16x8 of[16];
    const unsigned ooff = (unsigned)((p * 1024 + b * 128 + 32 * mg + m16) * 4096 + 512 * s8 + 256 * kq + 8 * g4) * 2u;
#define OL(i) do { asm volatile("buffer_load_dwordx4 %0, %1, %2, 0 offen offset:%3" : "=v"(of[2 * (i)]) : "v"(ooff), "s"(srdO), "i"(64 * (i)) : "memory"); \
                   asm volatile("buffer_load_dwordx4 %0, %1, %2, 0 offen offset:%3" : "=v"(of[2 * (i) + 1]) : "v"(ooff + 16u * 8192u), "s"(srdO), "i"(64 * (i)) : "memory"); } while (0)
    OL(0); OL(1); OL(2); OL(3); OL(4); OL(5); OL(6); OL(7);
    asm volatile("s_waitcnt vmcnt(0)\n\ts_barrier" : "+v"(of[0]), "+v"(of[1]), "+v"(of[2]), "+v"(of[3]), "+v"(of[4]), "+v"(of[5]), "+v"(of[6]), "+v"(of[7]) :: "memory");
    asm volatile("" : "+v"(of[8]), "+v"(of[9]), "+v"(of[10]), "+v"(of[11]), "+v"(of[12]), "+v"(of[13]), "+v"(of[14]), "+v"(of[15]) :: "memory");
    const int q4 = (lane & 15) >> 2, p4 = lane & 3;
    const unsigned ab0 = (unsigned)(16 * (g4 >> 1) + 0 + 4 * (g4 & 1) + q4) * 288u + 8u * p4;
    const unsigned ab1 = ab0 + 8u * 288u;
    f32x4 acc[2][9];
#pragma unroll
    for (int i = 0; i < 2; ++i)
#pragma unroll
        for (int c = 0; c < 9; ++c) acc[i][c] = f32x4{};
#pragma unroll
    for (int i = 0; i < 8; ++i) {
        const lds_cptr kb = L + (unsigned)(8 * kq + i) * (32u * 288u);
#pragma unroll
        for (int c = 0; c < 9; ++c) {
            const bf16x8 a = cat8(vtr(kb + ab0 + 32 * c), vtr(kb + ab1 + 32 * c));
            acc[0][c] = MFMA16(a, of[2 * i], acc[0][c]);
            acc[1][c] = MFMA16(a, of[2 * i + 1], acc[1][c]);
        }
    }
    asm volatile("s_waitcnt lgkmcnt(0)\n\ts_barrier" ::: "memory");
    const lds_ptr X = (lds_ptr)lds;
    const unsigned xw = (unsigned)w * 9216u + (unsigned)lane * 16u, xr = (unsigned)(w ^ 4) * 9216u + (unsigned)lane * 16u;
#pragma unroll
    for (int c = 0; c < 9; ++c) *(LAS f32x4*)(X + xw + 1024 * c) = kq ? acc[0][c] : acc[1][c];
    asm volatile("s_waitcnt lgkmcnt(0)\n\ts_barrier" ::: "memory");
    float* sl = slabs + ((size_t)((p * 8 + b) * 8 + s8) * 128 + 32 * mg + 16 * kq + m16) * 576 + 144 * c4 + 4 * g4;
#pragma unroll
    for (int c = 0; c < 9; ++c) {
        const f32x4 t4 = *(const LAS f32x4*)(X + xr + 1024 * c);
        const f32x4 mine = kq ? acc[1][c] : acc[0][c];
        *(f32x4*)(sl + 16 * c) = mine + t4;
    }
}

__global__ __launch_bounds__(256) void reduce_kernel(const float* __restrict__ slabs, unsigned short* __restrict__ out) {
    const int i = blockIdx.x * 256 + threadIdx.x;
    const int n4 = i % 144, row = (i / 144) & 1023, p = i / (144 * 1024);
    const int b = row >> 7, q = row & 127;
    const float* s = slabs + ((size_t)((p * 8 + b) * 8) * 128 + q) * 576 + 4 * n4;
    f32x4 a = *(const f32x4*)s;
#pragma unroll
    for (int k = 1; k < 8; ++k) a += *(const f32x4*)(s + (size_t)k * 128 * 576);
    u32x2 o = {cvtpk(a[0], a[1]), cvtpk(a[2], a[3])};
    *(u32x2*)(out + (size_t)row * 1152 + 576 * p + 4 * n4) = o;
}

extern "C" void kernel_launch(void* const* d_in, const int* in_sizes, int n_in, void* d_out, int out_size, void* d_ws, size_t ws_size, hipStream_t stream) {
    (void)hipFuncSetAttribute((const void*)attn_kernel, hipFuncAttributeMaxDynamicSharedMemorySize, LDS_BYTES);
    (void)hipFuncSetAttribute((const void*)oproj_kernel, hipFuncAttributeMaxDynamicSharedMemorySize, LDS_BYTES);
    char* Ows = (char*)d_ws;
    float* slabs = (float*)((char*)d_ws + (size_t)2048 * 4096 * 2);
    hipLaunchKernelGGL(attn_kernel, dim3(256), dim3(512), LDS_BYTES, stream,
                       (const char*)d_in[0], (const char*)d_in[1], (const char*)d_in[2], (const char*)d_in[3],
                       (const char*)d_in[5], (const char*)d_in[6], (const char*)d_in[7], (const char*)d_in[8], Ows);
    hipLaunchKernelGGL(oproj_kernel, dim3(512), dim3(512), 147456, stream, (const char*)Ows, (const char*)d_in[4], (const char*)d_in[9], slabs);
    hipLaunchKernelGGL(reduce_kernel, dim3(1152), dim3(256), 0, stream, (const float*)slabs, (unsigned short*)d_out);
}
```

```cpp
#include <hip/hip_runtime.h>
#include <hip/hip_bf16.h>
#include <cstdint>

typedef short bf16x8 __attribute__((ext_vector_type(8)));
typedef short s16x4 __attribute__((ext_vector_type(4)));
typedef float f32x16 __attribute__((ext_vector_type(16)));
typedef float f32x4 __attribute__((ext_vector_type(4)));
typedef float f32x2 __attribute__((ext_vector_type(2)));
typedef __bf16 bf16x2_t __attribute__((ext_vector_type(2)));
typedef unsigned u32x4 __attribute__((ext_vector_type(4)));
typedef unsigned u32x2 __attribute__((ext_vector_type(2)));
#define LAS __attribute__((address_space(3)))
#define GAS __attribute__((address_space(1)))
#define DI __device__ __forceinline__
typedef LAS const char* lds_cptr;
typedef LAS char* lds_ptr;

#define KVBYTES (8 * 32 * 1024 * 256)
constexpr int SLOT = 16384;
constexpr int LDS_BYTES = 163840;
constexpr float CSC = 0.08838834764831845f * 1.4426950408889634f;
constexpr float THR = 8.0f;
constexpr int S0 = 6 * SLOT, S1 = 8 * SLOT;
constexpr size_t WS_CNT = 0, WS_CNT_BYTES = 65536;
constexpr size_t WS_O = WS_CNT_BYTES, WS_O_BYTES = (size_t)2048 * 4096 * 2;
constexpr size_t WS_SL = WS_O + WS_O_BYTES, WS_SL_BYTES = (size_t)16 * 8 * 4 * 8 * 9 * 1024;
constexpr unsigned SPIN_CAP = 1u << 20;

#define MFMA32(a, b, c) __builtin_amdgcn_mfma_f32_32x32x16_bf16((a), (b), (c), 0, 0, 0)
#define MFMA16(a, b, c) __builtin_amdgcn_mfma_f32_16x16x32_bf16((a), (b), (c), 0, 0, 0)
#define RFL(x) __builtin_amdgcn_readfirstlane(x)
#define WAITBAR(N) asm volatile("s_waitcnt vmcnt(" #N ") lgkmcnt(0)\n\ts_barrier" ::: "memory")
#define LBAR() asm volatile("s_waitcnt lgkmcnt(0)\n\ts_barrier" ::: "memory")
#define SB() __builtin_amdgcn_sched_barrier(0)
#define PIN(x) asm volatile("" : "+v"(x))

DI unsigned cvtpk(float lo, float hi) { f32x2 v = {lo, hi}; bf16x2_t b = __builtin_convertvector(v, bf16x2_t); return __builtin_bit_cast(unsigned, b); }
DI s16x4 vtr(lds_cptr p) { return __builtin_bit_cast(s16x4, __builtin_amdgcn_ds_read_tr16_b64_v4i16((LAS s16x4*)p)); }
DI bf16x8 cat8(s16x4 a, s16x4 b) { return __builtin_shufflevector(a, b, 0, 1, 2, 3, 4, 5, 6, 7); }
DI bf16x8 ldsrd128(lds_cptr p) { return *(const LAS bf16x8*)p; }
DI float swap32max(float m) { auto rr = __builtin_amdgcn_permlane32_swap(__float_as_uint(m), __float_as_uint(m), false, false); return __builtin_fmaxf(__uint_as_float(rr[0]), __uint_as_float(rr[1])); }
DI float swap32sum(float m) { auto rr = __builtin_amdgcn_permlane32_swap(__float_as_uint(m), __float_as_uint(m), false, false); return __uint_as_float(rr[0]) + __uint_as_float(rr[1]); }
DI bf16x8 pack8s(const f32x16& a, int sg, float sc) {
    u32x4 wv = {cvtpk(a[8 * sg + 0] * sc, a[8 * sg + 1] * sc), cvtpk(a[8 * sg + 2] * sc, a[8 * sg + 3] * sc), cvtpk(a[8 * sg + 4] * sc, a[8 * sg + 5] * sc), cvtpk(a[8 * sg + 6] * sc, a[8 * sg + 7] * sc)};
    return __builtin_bit_cast(bf16x8, wv);
}
#define BC8(x) __builtin_bit_cast(bf16x8, x)

DI void dma16_nt(unsigned ldsdst, unsigned voff, __amdgpu_buffer_rsrc_t srd, unsigned soff) {
    unsigned sv;
    asm volatile("s_mov_b32 %0, m0\n\ts_mov_b32 m0, %1\n\ts_nop 0\n\tbuffer_load_dwordx4 %2, %3, %4 offen nt lds\n\ts_mov_b32 m0, %0"
                 : "=&s"(sv) : "s"(ldsdst), "v"(voff), "s"(srd), "s"(soff) : "memory");
}
DI void dma16(unsigned ldsdst, unsigned voff, __amdgpu_buffer_rsrc_t srd, unsigned soff) {
    unsigned sv;
    asm volatile("s_mov_b32 %0, m0\n\ts_mov_b32 m0, %1\n\ts_nop 0\n\tbuffer_load_dwordx4 %2, %3, %4 offen lds\n\ts_mov_b32 m0, %0"
                 : "=&s"(sv) : "s"(ldsdst), "v"(voff), "s"(srd), "s"(soff) : "memory");
}
DI void st16_sc1(u32x4 v, unsigned voff, __amdgpu_buffer_rsrc_t srd, unsigned soff) {
    __builtin_amdgcn_raw_buffer_store_b128(v, srd, (int)(voff + soff), 0, 16);
}
#define LD16_SC1(dst, voff, srd, soff) asm volatile("buffer_load_dwordx4 %0, %1, %2, %3 offen sc1" : "=v"(dst) : "v"(voff), "s"(srd), "s"(soff) : "memory")
typedef GAS unsigned gu32;
DI void wait_cnt(gu32* c, unsigned want) {
    for (unsigned sp = 0; sp < SPIN_CAP; ++sp) {
        if ((unsigned)RFL(__hip_atomic_load(c, __ATOMIC_RELAXED, __HIP_MEMORY_SCOPE_AGENT)) >= want) break;
        __builtin_amdgcn_s_sleep(1);
    }
    asm volatile("" ::: "memory");
}

#define STAMP(i)
#define STAMPT(T)

__global__ __launch_bounds__(512, 2) void fused_kernel(const char* __restrict__ xA, const char* __restrict__ kA, const char* __restrict__ vA, const char* __restrict__ wqA, const char* __restrict__ woA,
                                                      const char* __restrict__ xB, const char* __restrict__ kB, const char* __restrict__ vB, const char* __restrict__ wqB, const char* __restrict__ woB,
                                                      char* __restrict__ ws, unsigned short* __restrict__ out) {
    extern __shared__ __attribute__((aligned(16))) char lds[];
    const int tid = threadIdx.x, lane = tid & 63;
    const int w = RFL(tid >> 6);
    const int r32 = lane & 31, hi = lane >> 5;
    const int wg = blockIdx.x, xcd = wg & 7, idx = wg >> 3;
    const int p = xcd & 1, h = 8 * (xcd >> 1) + (idx & 7), bq = idx >> 3;
    const int s8 = h >> 2, c4 = h & 3;
    const unsigned lds0 = (unsigned)(uintptr_t)lds;
    const lds_cptr L = (lds_cptr)lds;
    const lds_ptr LW = (lds_ptr)lds;
    STAMP(0);
    const __amdgpu_buffer_rsrc_t srdX = __builtin_amdgcn_make_buffer_rsrc((void*)(p ? xB : xA), (short)0, 8 * 128 * 576 * 2, 0x00020000);
    const __amdgpu_buffer_rsrc_t srdW = __builtin_amdgcn_make_buffer_rsrc((void*)(p ? wqB : wqA), (short)0, 576 * 4096 * 2, 0x00020000);
    const __amdgpu_buffer_rsrc_t srdK = __builtin_amdgcn_make_buffer_rsrc((void*)(p ? kB : kA), (short)0, KVBYTES, 0x00020000);
    const __amdgpu_buffer_rsrc_t srdV = __builtin_amdgcn_make_buffer_rsrc((void*)(p ? vB : vA), (short)0, KVBYTES, 0x00020000);
    const __amdgpu_buffer_rsrc_t srdWo = __builtin_amdgcn_make_buffer_rsrc((void*)(p ? woB : woA), (short)0, 4096 * 576 * 2, 0x00020000);
    const __amdgpu_buffer_rsrc_t srdO = __builtin_amdgcn_make_buffer_rsrc((void*)(ws + WS_O), (short)0, (int)WS_O_BYTES, 0x00020000);
    const __amdgpu_buffer_rsrc_t srdS = __builtin_amdgcn_make_buffer_rsrc((void*)(ws + WS_SL), (short)0, (int)WS_SL_BYTES, 0x00020000);
    gu32* cnt = (gu32*)(ws + WS_CNT);
    auto cntO = [&](int r) { return cnt + 16 * (((r * 2 + p) * 4 + bq) * 8 + s8); };
    auto cntS = [&](int r) { return cnt + 16 * (128 + ((r * 2 + p) * 4 + bq) * 4 + c4); };

    const bool isB = w >= 4;
    const int bw = w & 3;
    const int r7 = (lane >> 2) & 7;
    const int blk = (lane >> 4) & 1, q4 = (lane & 15) >> 2, p4 = lane & 3;
    const unsigned chs0 = 16u * (4u * (unsigned)hi + (unsigned)((lane & 3) ^ ((2 * (2 * bw) + (r7 >> 2)) & 3)));
    const unsigned chs1 = 16u * (4u * (unsigned)hi + (unsigned)((lane & 3) ^ ((2 * (2 * bw + 1) + (r7 >> 2)) & 3)));
    const unsigned srcKV0 = (unsigned)(16 * bw + r7) * 256u + chs0, srcKV1 = (unsigned)(16 * bw + 8 + r7) * 256u + chs1;
    const unsigned srcW0 = (unsigned)(16 * bw + r7) * 8192u + chs0, srcW1 = (unsigned)(16 * bw + 8 + r7) * 8192u + chs1;
    const unsigned srcXe = (unsigned)(32 * bw + (lane >> 3)) * 1152u + 16u * (unsigned)((lane & 7) ^ ((lane >> 4) & 7));
    const unsigned srcXo = (unsigned)(32 * bw + 8 + (lane >> 3)) * 1152u + 16u * (unsigned)((lane & 7) ^ ((4 + (lane >> 4)) & 7));
    const unsigned dstB = lds0 + 4096u * (unsigned)bw;

    auto kv_off = [&](int T) -> unsigned { const int r = T >> 4, t = T & 15; return (unsigned)(((bq + 4 * r) * 32 + h) * 1024 + 64 * t) * 256u; };
    auto kslot = [&](int T) -> unsigned { return (unsigned)(T % 3) * (unsigned)SLOT; };
    auto vslot = [&](int T) -> unsigned { return (unsigned)(3 + T % 3) * (unsigned)SLOT; };
    auto dma_tile = [&](__amdgpu_buffer_rsrc_t srd, int T, unsigned slot) { const unsigned so = kv_off(T), d = dstB + slot;
        dma16_nt(RFL(d), srcKV0, srd, RFL(so)); dma16_nt(RFL(d + 1024), srcKV0, srd, RFL(so + 128));
        dma16_nt(RFL(d + 2048), srcKV1, srd, RFL(so)); dma16_nt(RFL(d + 3072), srcKV1, srd, RFL(so + 128)); };
    auto dma_k = [&](int T) { dma_tile(srdK, T, kslot(T)); };
    auto dma_v = [&](int T) { dma_tile(srdV, T, vslot(T)); };
    auto dma_chunk = [&](int b, int c, unsigned xs, unsigned wsl) {
        const unsigned sx = (unsigned)(b * 128) * 1152u + 128u * c, sw = (unsigned)(64 * c) * 8192u + (unsigned)h * 256u;
        const unsigned dx = dstB + xs, dw = dstB + wsl;
        dma16(RFL(dx), srcXe, srdX, RFL(sx)); dma16(RFL(dx + 1024), srcXo, srdX, RFL(sx));
        dma16(RFL(dx + 2048), srcXe, srdX, RFL(sx + 16u * 1152u)); dma16(RFL(dx + 3072), srcXo, srdX, RFL(sx + 16u * 1152u));
        dma16(RFL(dw), srcW0, srdW, RFL(sw)); dma16(RFL(dw + 1024), srcW0, srdW, RFL(sw + 128));
        dma16(RFL(dw + 2048), srcW1, srdW, RFL(sw)); dma16(RFL(dw + 3072), srcW1, srdW, RFL(sw + 128));
    };
    const unsigned wb0 = 2048u * hi + 64u * (0 + q4) + 16u * ((2 * blk + (p4 & 1)) ^ (2 * hi + 0)) + 8u * (p4 >> 1);
    const unsigned wb1 = 2048u * hi + 64u * (4 + q4) + 16u * ((2 * blk + (p4 & 1)) ^ (2 * hi + 1)) + 8u * (p4 >> 1);
    const unsigned xb32 = (unsigned)r32 * 128u + 16u * (unsigned)(hi ^ ((r32 >> 1) & 7));

    constexpr unsigned Q1X[3] = {2u * SLOT, (unsigned)S0, (unsigned)S1}, Q1W[3] = {5u * SLOT, (unsigned)S0 + SLOT, (unsigned)S1 + SLOT};
    if (isB) { dma_chunk(bq, 0, Q1X[0], Q1W[0]); dma_chunk(bq, 1, Q1X[1], Q1W[1]); dma_chunk(bq, 2, Q1X[2], Q1W[2]); dma_k(0); dma_v(0); dma_k(1); dma_v(1); }
    {
        const int dh = w & 1, qb = w >> 1;
        f32x16 acc[2]; acc[0] = f32x16{}; acc[1] = f32x16{};
        STAMP(1);
#pragma unroll 1
        for (int c = 0; c < 9; ++c) {
            const int c3 = c % 3;
            if (isB) {
                if (c == 0) WAITBAR(32); else if (c <= 2) WAITBAR(24); else if (c < 8) WAITBAR(8); else WAITBAR(0);
                if (c >= 1 && c <= 6) { const int n3 = (c + 2) % 3; dma_chunk(bq, c + 2, n3 == 0 ? 2u * SLOT : n3 == 1 ? (unsigned)S0 : (unsigned)S1, n3 == 0 ? 5u * SLOT : n3 == 1 ? (unsigned)S0 + SLOT : (unsigned)S1 + SLOT); }
            } else { LBAR(); }
            const unsigned xo = c3 == 0 ? 2u * SLOT : c3 == 1 ? (unsigned)S0 : (unsigned)S1, wo_ = c3 == 0 ? 5u * SLOT : c3 == 1 ? (unsigned)S0 + SLOT : (unsigned)S1 + SLOT;
            unsigned xa = lds0 + xo + 4096u * qb + xb32, wa0 = lds0 + wo_ + 1024u * dh + wb0, wa1 = lds0 + wo_ + 1024u * dh + wb1;
            asm volatile("" : "+v"(xa), "+v"(wa0), "+v"(wa1));
            const lds_cptr w0p = (lds_cptr)(uintptr_t)wa0, w1p = (lds_cptr)(uintptr_t)wa1;
            bf16x8 a0_[4], a1_[4], b0_[4];
#pragma unroll
            for (int ks = 0; ks < 4; ++ks) {
                a0_[ks] = cat8(vtr(w0p + 4096 * ks), vtr(w1p + 4096 * ks));
                b0_[ks] = ldsrd128((lds_cptr)(uintptr_t)(xa ^ (32u * ks)));
                a1_[ks] = cat8(vtr(w0p + 4096 * ks + 512), vtr(w1p + 4096 * ks + 512));
            }
            SB();
#pragma unroll
            for (int ks = 0; ks < 4; ++ks) { acc[0] = MFMA32(a0_[ks], b0_[ks], acc[0]); acc[1] = MFMA32(a1_[ks], b0_[ks], acc[1]); }
        }
        STAMP(3);
#pragma unroll
        for (int i = 0; i < 2; ++i)
#pragma unroll
            for (int sg = 0; sg < 2; ++sg) *(LAS bf16x8*)(LW + S0 + (qb * 8 + 4 * dh + 2 * i + sg) * 1024 + lane * 16) = pack8s(acc[i], sg, CSC);
        LBAR();
    }

    const int g4 = lane >> 4;
    const unsigned lo16 = (unsigned)lane * 16u;
    const unsigned ab0 = (unsigned)(16 * (g4 >> 1) + 0 + 4 * (g4 & 1) + q4) * 288u + 8u * p4;
    const unsigned ab1 = ab0 + 8u * 288u;
    constexpr unsigned WSB = 108544u, WCH = 18432u;
    if (isB) {
        {
            const int dh = bw & 1, qg = bw >> 1;
            f32x16 acc[2][2];
#pragma unroll
            for (int i = 0; i < 2; ++i)
#pragma unroll
                for (int j = 0; j < 2; ++j) acc[i][j] = f32x16{};
            int nprev = 0;
#pragma unroll 1
            for (int T = 0; T < 16; ++T) {
                if (nprev == 8) WAITBAR(8); else if (nprev == 16) WAITBAR(16); else if (nprev == 12) WAITBAR(12); else WAITBAR(0);
                STAMPT(T);
                const int q3 = T / 3, m3 = T - 3 * q3;
                int ci = (m3 == 0) ? 2 * q3 : (m3 == 1) ? 2 * q3 + 1 : -1; if (ci > 8) ci = -1;
                int cc = (m3 == 2) ? 2 * q3 : (m3 == 0) ? 2 * q3 - 1 : -1; if (cc > 8) cc = -1;
                dma_k(T + 2); if (T > 0) dma_v(T + 1);
                nprev = (T > 0) ? 8 : 4;
                if (ci >= 0) { const unsigned sb = (ci & 1) ? S1 : S0; dma_chunk(bq + 4, ci, sb, sb + SLOT); nprev += 8; }
                if (cc >= 0) {
                    const unsigned sb = (cc & 1) ? S1 : S0;
                    unsigned xa = lds0 + sb + 8192u * qg + xb32, wa0 = lds0 + sb + SLOT + 1024u * dh + wb0, wa1 = lds0 + sb + SLOT + 1024u * dh + wb1;
                    asm volatile("" : "+v"(xa), "+v"(wa0), "+v"(wa1));
                    const lds_cptr w0p = (lds_cptr)(uintptr_t)wa0, w1p = (lds_cptr)(uintptr_t)wa1;
                    bf16x8 a0_[4], a1_[4], b0_[4], b1_[4];
#pragma unroll
                    for (int ks = 0; ks < 4; ++ks) {
                        a0_[ks] = cat8(vtr(w0p + 4096 * ks), vtr(w1p + 4096 * ks));
                        b0_[ks] = ldsrd128((lds_cptr)(uintptr_t)(xa ^ (32u * ks)));
                        b1_[ks] = ldsrd128((lds_cptr)(uintptr_t)((xa ^ (32u * ks)) + 4096u));
                        a1_[ks] = cat8(vtr(w0p + 4096 * ks + 512), vtr(w1p + 4096 * ks + 512));
                    }
                    SB();
#pragma unroll
                    for (int ks = 0; ks < 4; ++ks) {
                        acc[0][0] = MFMA32(a0_[ks], b0_[ks], acc[0][0]); acc[0][1] = MFMA32(a0_[ks], b1_[ks], acc[0][1]);
                        acc[1][0] = MFMA32(a1_[ks], b0_[ks], acc[1][0]); acc[1][1] = MFMA32(a1_[ks], b1_[ks], acc[1][1]);
                    }
                    if (cc == 8) {
#pragma unroll
                        for (int i = 0; i < 2; ++i)
#pragma unroll
                            for (int j = 0; j < 2; ++j)
#pragma unroll
                                for (int sg = 0; sg < 2; ++sg) *(LAS bf16x8*)(LW + S1 + ((2 * qg + j) * 8 + 4 * dh + 2 * i + sg) * 1024 + lane * 16) = pack8s(acc[i][j], sg, CSC);
                    }
                }
            }
        }
        {
            unsigned wov[5];
#pragma unroll
            for (int i = 0; i < 5; ++i) {
                const int pc = (i < 4) ? bw + 4 * i : 16 + (bw & 1);
                const int uu = 64 * pc + lane, pos = uu / 18, ch = uu - 18 * pos;
                const int k = (pos & ~12) | ((pos & 4) << 1) | ((pos & 8) >> 1);
                wov[i] = (unsigned)k * 1152u + 16u * ch;
            }
            auto dma_wo = [&](int j, unsigned dst) {
                const unsigned so = (unsigned)(512 * s8 + 64 * j) * 1152u + 288u * c4;
#pragma unroll
                for (int i = 0; i < 5; ++i) { const int pc = (i < 4) ? bw + 4 * i : 16 + (bw & 1); dma16(RFL(lds0 + dst + 1024u * pc), wov[i], srdWo, RFL(so)); }
            };
            f32x4 acc[2][9];
#pragma unroll
            for (int i = 0; i < 2; ++i)
#pragma unroll
                for (int c = 0; c < 9; ++c) acc[i][c] = f32x4{};
            bf16x8 ofx[4], ofy[4];
            unsigned flagv = 0;
#pragma unroll 1
            for (int T = 16; T < 21; ++T) {
                if (T == 18 || T == 20) WAITBAR(9); else WAITBAR(8);
                STAMPT(T);
                if (T == 17) { if (lane == 0) __hip_atomic_fetch_add(cntO(0), 1u, __ATOMIC_RELAXED, __HIP_MEMORY_SCOPE_AGENT); }
                if (T == 19) asm volatile("global_load_dword %0, %1, off sc1" : "=v"(flagv) : "v"(cntO(0)) : "memory");
                dma_k(T + 2); dma_v(T + 1);
            }
#define O1L(OF, j) do { const unsigned so_ = (unsigned)((((p * 8 + bq) * 32 + 4 * s8 + ((j) >> 1)) * 8 + 2 * bw) * 4 + 2 * ((j) & 1)) * 1024u; \
                LD16_SC1(OF[0], lo16, srdO, RFL(so_)); LD16_SC1(OF[1], lo16, srdO, RFL(so_ + 4096u)); LD16_SC1(OF[2], lo16, srdO, RFL(so_ + 1024u)); LD16_SC1(OF[3], lo16, srdO, RFL(so_ + 5120u)); } while (0)
#define OPCOMP(pos, OF) do { \
                asm volatile("" : "+v"(OF[0]), "+v"(OF[1]), "+v"(OF[2]), "+v"(OF[3]) :: "memory"); \
                unsigned a0a = lds0 + WSB + WCH * (unsigned)((pos) % 3) + ab0; asm volatile("" : "+v"(a0a)); \
                const lds_cptr kb_ = (lds_cptr)(uintptr_t)a0a; \
                bf16x8 af_[18]; \
                _Pragma("unroll") for (int ks = 0; ks < 2; ++ks) _Pragma("unroll") for (int c = 0; c < 9; ++c) af_[9 * ks + c] = cat8(vtr(kb_ + 9216 * ks + 32 * c), vtr(kb_ + 9216 * ks + 2304 + 32 * c)); \
                SB(); \
                _Pragma("unroll") for (int ks = 0; ks < 2; ++ks) _Pragma("unroll") for (int c = 0; c < 9; ++c) { \
                    acc[0][c] = MFMA16(af_[9 * ks + c], OF[2 * ks], acc[0][c]); acc[1][c] = MFMA16(af_[9 * ks + c], OF[2 * ks + 1], acc[1][c]); } \
            } while (0)
#define OPSTEP(T, OF) do { \
                const int ii = (T) - 21, ic = (T) - 23; \
                if ((T) == 21) WAITBAR(8); else if ((T) <= 29) WAITBAR(17); else WAITBAR(8); \
                STAMPT(T); \
                if ((T) == 21) { asm volatile("" : "+v"(flagv) :: "memory"); if ((unsigned)RFL(flagv) < 16u) wait_cnt(cntO(0), 16u); } \
                if (ii <= 7) dma_wo((ii + 3) & 7, WSB + WCH * (unsigned)(ii % 3)); \
                if ((T) + 2 < 32) dma_k((T) + 2); dma_v((T) + 1); \
                if (ic >= 0) OPCOMP(ic, OF); \
                if (ii <= 7) O1L(OF, (ii + 3) & 7); \
            } while (0)
#pragma unroll 1
            for (int T = 21; T < 31; T += 2) { OPSTEP(T, ofx); OPSTEP(T + 1, ofy); }
            {
                const unsigned sso = (unsigned)(((((p * 8 + bq) * 8 + s8) * 4 + c4) * 8 + 2 * bw) * 9) * 1024u;
#pragma unroll
                for (int i = 0; i < 2; ++i)
#pragma unroll
                    for (int c = 0; c < 9; ++c) st16_sc1(__builtin_bit_cast(u32x4, acc[i][c]), lo16, srdS, RFL(sso + 1024u * (9 * i + c)));
            }
            WAITBAR(18);
            WAITBAR(0);  STAMPT(32);
            if (lane == 0) __hip_atomic_fetch_add(cntS(0), 1u, __ATOMIC_RELAXED, __HIP_MEMORY_SCOPE_AGENT);
            dma_wo(3, 0u); dma_wo(4, WCH); dma_wo(5, 2u * WCH); dma_wo(6, 81920u);
            LBAR();
            dma_wo(7, 3u * WCH);
        }
    } else {
        const int qb = w;
        const unsigned qpark = (unsigned)(qb * 8) * 1024u + (unsigned)lane * 16u;
        bf16x8 qf[8];
#pragma unroll
        for (int s = 0; s < 8; ++s) qf[s] = ldsrd128(L + S0 + qpark + 1024 * s);
        const unsigned kb0 = 2048u * (r32 >> 3) + 64u * (r32 & 7) + 16u * ((0 + hi) ^ ((r32 >> 2) & 3));
        const unsigned kb1 = 2048u * (r32 >> 3) + 64u * (r32 & 7) + 16u * ((2 + hi) ^ ((r32 >> 2) & 3));
        const unsigned vb0 = 64u * (4 * hi + q4) + 16u * ((2 * blk + (p4 >> 1)) ^ (0 + hi)) + 8u * (p4 & 1);
        const unsigned vb1 = 2048u + 64u * (4 * hi + q4) + 16u * ((2 * blk + (p4 >> 1)) ^ (2 + hi)) + 8u * (p4 & 1);
        f32x16 o[4];
#pragma unroll
        for (int i = 0; i < 4; ++i) o[i] = f32x16{};
        float lsum = 0.f, fpend = 1.f; bool pend = false;
        f32x16 sA = f32x16{}, sB = f32x16{}, negm = f32x16{};
        u32x4 pA0 = {}, pA1 = {}, pB0 = {}, pB1 = {};
#define KRD(kp, ks) ldsrd128((((ks) & 1) ? k1p : k0p) + (kp) + 512 * ((ks) >> 1))
#define VRD(vp, sg, db) cat8(vtr(v0p + (vp) + 4096 * (sg) + 512 * (db)), vtr(v1p + (vp) + 4096 * (sg) + 512 * (db)))
#define BASES(T) unsigned k0a = lds0 + kb0 + kslot(T), k1a = lds0 + kb1 + kslot(T), v0a = lds0 + vb0 + vslot((T) + 2), v1a = lds0 + vb1 + vslot((T) + 2); \
            asm volatile("" : "+v"(k0a), "+v"(k1a), "+v"(v0a), "+v"(v1a)); \
            const lds_cptr k0p = (lds_cptr)(uintptr_t)k0a, k1p = (lds_cptr)(uintptr_t)k1a, v0p = (lds_cptr)(uintptr_t)v0a, v1p = (lds_cptr)(uintptr_t)v1a
#define EX1(S, i) do { S[i] = __builtin_amdgcn_exp2f(S[i]); } while (0)
#define ROWMAX(S, rm) do { float a_ = __builtin_fmaxf(__builtin_fmaxf(S[0], S[1]), S[2]), b_ = __builtin_fmaxf(__builtin_fmaxf(S[3], S[4]), S[5]); \
            a_ = __builtin_fmaxf(__builtin_fmaxf(a_, S[6]), S[7]); b_ = __builtin_fmaxf(__builtin_fmaxf(b_, S[8]), S[9]); \
            a_ = __builtin_fmaxf(__builtin_fmaxf(a_, S[10]), S[11]); b_ = __builtin_fmaxf(__builtin_fmaxf(b_, S[12]), S[13]); \
            a_ = __builtin_fmaxf(__builtin_fmaxf(a_, S[14]), S[15]); rm = swap32max(__builtin_fmaxf(a_, b_)); } while (0)
#define APPLY_PEND() do { if (__builtin_expect(pend, 0)) { _Pragma("unroll") for (int d = 0; d < 4; ++d) _Pragma("unroll") for (int i = 0; i < 16; ++i) o[d][i] *= fpend; pend = false; } } while (0)
#define DECIDE(SN, g_) do { \
            if (__builtin_expect(__any(g_ > THR), 0)) { const float dl_ = __builtin_fmaxf(g_, 0.f); \
                _Pragma("unroll") for (int i = 0; i < 16; ++i) { SN[i] -= dl_; negm[i] -= dl_; } \
                fpend = __builtin_amdgcn_exp2f(-dl_); lsum *= fpend; pend = true; } \
        } while (0)
#define RMAX1(S, a_, b_) do { a_ = __builtin_fmaxf(__builtin_fmaxf(S[0], S[1]), S[2]); b_ = __builtin_fmaxf(__builtin_fmaxf(S[3], S[4]), S[5]); \
            a_ = __builtin_fmaxf(__builtin_fmaxf(a_, S[6]), S[7]); b_ = __builtin_fmaxf(__builtin_fmaxf(b_, S[8]), S[9]); } while (0)
#define RMAX2(S, a_, b_, rm) do { a_ = __builtin_fmaxf(__builtin_fmaxf(a_, S[10]), S[11]); b_ = __builtin_fmaxf(__builtin_fmaxf(b_, S[12]), S[13]); \
            a_ = __builtin_fmaxf(__builtin_fmaxf(a_, S[14]), S[15]); rm = swap32max(__builtin_fmaxf(a_, b_)); } while (0)
#define SLOT_TAIL(SC, SN, PP1, PC0, PC1, kp, vp, DOQK, DOPV) \
              if (DOQK) { SN = MFMA32(k0_, qf[0], negm); k0_ = KRD(kp, 3); } EX1(SC, 0); EX1(SC, 1); PIN(SC); SB(); \
              if (DOPV) { o[0] = MFMA32(v0_, BC8(PP1), o[0]); v0_ = VRD(vp, 1, 3); } EX1(SC, 2); s0_ = SC[0] + SC[1]; PIN(SC); PIN(s0_); SB(); \
              if (DOQK) { SN = MFMA32(k1_, qf[1], SN); k1_ = KRD(kp, 4); } EX1(SC, 3); EX1(SC, 4); PC0[0] = cvtpk(SC[0], SC[1]); PIN(SC); PIN(PC0); SB(); \
              if (DOPV) o[1] = MFMA32(v1_, BC8(PP1), o[1]); EX1(SC, 5); s1_ = SC[2] + SC[3]; s0_ += SC[4]; PIN(SC); PIN(s0_); PIN(s1_); SB(); \
              if (DOQK) { SN = MFMA32(k2_, qf[2], SN); k2_ = KRD(kp, 5); } EX1(SC, 6); EX1(SC, 7); PC0[1] = cvtpk(SC[2], SC[3]); PIN(SC); PIN(PC0); SB(); \
              if (DOPV) o[2] = MFMA32(v2_, BC8(PP1), o[2]); EX1(SC, 8); s1_ += SC[5]; s0_ += SC[6]; PIN(SC); PIN(s0_); PIN(s1_); SB(); \
              if (DOQK) { SN = MFMA32(k0_, qf[3], SN); k0_ = KRD(kp, 6); } EX1(SC, 9); EX1(SC, 10); PC0[2] = cvtpk(SC[4], SC[5]); PIN(SC); PIN(PC0); SB(); \
              if (DOPV) o[3] = MFMA32(v0_, BC8(PP1), o[3]); EX1(SC, 11); s1_ += SC[7]; s0_ += SC[8]; PIN(SC); PIN(s0_); PIN(s1_); SB(); \
              if (DOQK) { SN = MFMA32(k1_, qf[4], SN); k1_ = KRD(kp, 7); } EX1(SC, 12); EX1(SC, 13); PC0[3] = cvtpk(SC[6], SC[7]); PIN(SC); PIN(PC0); SB(); \
              if (DOQK) SN = MFMA32(k2_, qf[5], SN); EX1(SC, 14); s1_ += SC[9]; s0_ += SC[10]; PC1[0] = cvtpk(SC[8], SC[9]); PIN(SC); PIN(s0_); PIN(s1_); PIN(PC1); SB(); \
              if (DOQK) SN = MFMA32(k0_, qf[6], SN); EX1(SC, 15); s1_ += SC[11]; s0_ += SC[12]; PC1[1] = cvtpk(SC[10], SC[11]); PIN(SC); PIN(s0_); PIN(s1_); PIN(PC1); SB(); \
              if (DOQK) SN = MFMA32(k1_, qf[7], SN); s1_ += SC[13]; s0_ += SC[14]; PC1[2] = cvtpk(SC[12], SC[13]); PIN(s0_); PIN(s1_); PIN(PC1); SB(); \
            s1_ += SC[15]; PC1[3] = cvtpk(SC[14], SC[15]); lsum += s0_ + s1_; PIN(PC1); PIN(lsum); SB();
#define SLOTM(SC, SN, PP0, PP1, PC0, PC1, kp, vp, DOQK, DOPV, FIRST) do { \
            float rm_ = 0.f, ma_, mb_; float s0_ = 0.f, s1_ = 0.f; \
            bf16x8 v0_, v1_, v2_, v3_, k0_, k1_, k2_; \
            if (DOPV) { v0_ = VRD(vp, 0, 0); v1_ = VRD(vp, 0, 1); v2_ = VRD(vp, 0, 2); v3_ = VRD(vp, 0, 3); } \
            if (DOQK) { k0_ = KRD(kp, 0); k1_ = KRD(kp, 1); k2_ = KRD(kp, 2); } \
            SB(); \
            APPLY_PEND(); \
            if (FIRST) { RMAX1(SC, ma_, mb_); RMAX2(SC, ma_, mb_, rm_); DECIDE(SC, rm_); SB(); } \
              if (DOPV) { o[0] = MFMA32(v0_, BC8(PP0), o[0]); v0_ = VRD(vp, 1, 0); } SB(); \
              if (DOPV) { o[1] = MFMA32(v1_, BC8(PP0), o[1]); v1_ = VRD(vp, 1, 1); } SB(); \
              if (DOPV) { o[2] = MFMA32(v2_, BC8(PP0), o[2]); v2_ = VRD(vp, 1, 2); } if (!(FIRST)) { RMAX1(SC, ma_, mb_); PIN(ma_); PIN(mb_); } SB(); \
              if (DOPV) o[3] = MFMA32(v3_, BC8(PP0), o[3]); if (!(FIRST)) { RMAX2(SC, ma_, mb_, rm_); PIN(rm_); } SB(); \
            if (!(FIRST)) { DECIDE(SC, rm_); SB(); } \
            SLOT_TAIL(SC, SN, PP1, PC0, PC1, kp, vp, DOQK, DOPV) \
        } while (0)

        WAITBAR(0);
        STAMP(8);
#pragma unroll 1
        for (int r = 0; r < 2; ++r) {
            {
                const int T = 16 * r;
                BASES(T);
#pragma unroll
                for (int ks = 0; ks < 8; ++ks) sA = MFMA32(KRD(0, ks), qf[ks], ks ? sA : (f32x16{}));
                float rm; ROWMAX(sA, rm); pend = false;
#pragma unroll
                for (int i = 0; i < 16; ++i) { sA[i] -= rm; negm[i] = -rm; }
                SLOTM(sA, sB, pB0, pB1, pA0, pA1, 8192, 0, true, false, true);
            }
#pragma unroll 1
            for (int t = 1; t < 16; ++t) {
                const int T = 16 * r + t;
                WAITBAR(0);
                STAMPT(T);
                BASES(T);
                SLOTM(sB, sA, pA0, pA1, pB0, pB1, 0, 0, true, true, true);
                SLOTM(sA, sB, pB0, pB1, pA0, pA1, 8192, 8192, true, true, false);
            }
            {
                const int T = 16 * r + 16;
                WAITBAR(0);
                STAMPT(T);
                BASES(T);
                SLOTM(sB, sA, pA0, pA1, pB0, pB1, 0, 0, false, true, true);
                APPLY_PEND();
#pragma unroll
                for (int sg = 0; sg < 2; ++sg)
#pragma unroll
                    for (int db = 0; db < 4; ++db) o[db] = MFMA32(VRD(8192, sg, db), BC8(sg ? pB1 : pB0), o[db]);
                const float inv = 1.0f / swap32sum(lsum);
                int ln = lane; asm volatile("" : "+v"(ln));
                const unsigned ovo = (unsigned)((ln >> 4) & 1) * 4096u + (unsigned)(ln & 15) * 16u + (unsigned)(ln >> 5) * 256u;
                const unsigned oso = (unsigned)((((p * 8 + bq + 4 * r) * 32 + h) * 8 + 2 * qb) * 4) * 1024u;
#pragma unroll
                for (int db = 0; db < 4; ++db)
#pragma unroll
                    for (int g = 0; g < 4; g += 2) {
                        unsigned a0 = cvtpk(o[db][4 * g] * inv, o[db][4 * g + 1] * inv), a1 = cvtpk(o[db][4 * g + 2] * inv, o[db][4 * g + 3] * inv);
                        unsigned b0 = cvtpk(o[db][4 * g + 4] * inv, o[db][4 * g + 5] * inv), b1 = cvtpk(o[db][4 * g + 6] * inv, o[db][4 * g + 7] * inv);
                        auto r0 = __builtin_amdgcn_permlane32_swap(a0, b0, false, false); auto r1 = __builtin_amdgcn_permlane32_swap(a1, b1, false, false);
                        const u32x4 v4 = {r0[0], r1[0], r0[1], r1[1]};
                        st16_sc1(v4, ovo, srdO, RFL(oso + 1024u * db + 256u * g));
                    }
#pragma unroll
                for (int i = 0; i < 4; ++i) o[i] = f32x16{};
                lsum = 0.f;
                if (r == 0) {
#pragma unroll
                    for (int s2 = 0; s2 < 8; ++s2) qf[s2] = ldsrd128(L + S1 + qpark + 1024 * s2);
                }
            }
        }
        WAITBAR(0);
    }
    STAMP(4);
    if (tid == 0) __hip_atomic_fetch_add(cntO(1), 1u, __ATOMIC_RELAXED, __HIP_MEMORY_SCOPE_AGENT);
    {
        auto reduce_round = [&](int r) {
            const int b = bq + 4 * r;
#pragma unroll 1
            for (int ct = w; ct < 9; ct += 8) {
                f32x4 sv[8];
#pragma unroll
                for (int k = 0; k < 8; ++k) LD16_SC1(sv[k], lo16, srdS, RFL((unsigned)(((((p * 8 + b) * 8 + k) * 4 + c4) * 8 + s8) * 9 + ct) * 1024u));
                asm volatile("s_waitcnt vmcnt(0)" : "+v"(sv[0]), "+v"(sv[1]), "+v"(sv[2]), "+v"(sv[3]), "+v"(sv[4]), "+v"(sv[5]), "+v"(sv[6]), "+v"(sv[7]) :: "memory");
                const f32x4 a = ((sv[0] + sv[1]) + (sv[2] + sv[3])) + ((sv[4] + sv[5]) + (sv[6] + sv[7]));
                const u32x2 ov = {cvtpk(a[0], a[1]), cvtpk(a[2], a[3])};
                *(u32x2*)(out + (size_t)(b * 128 + 16 * s8 + (lane & 15)) * 1152 + 576 * p + 144 * c4 + 16 * ct + 4 * g4) = ov;
            }
        };
        wait_cnt(cntS(0), 32u);
        reduce_round(0);
        STAMP(7);
        const int mg = w & 3, kq = w >> 2;
        wait_cnt(cntO(1), 4u);
        bf16x8 of[16];
#pragma unroll
        for (int i = 0; i < 8; ++i) {
            const unsigned so = (unsigned)((((p * 8 + bq + 4) * 32 + 4 * s8 + 2 * kq + (i >> 2)) * 8 + 2 * mg) * 4 + (i & 3)) * 1024u;
            LD16_SC1(of[2 * i], lo16, srdO, RFL(so)); LD16_SC1(of[2 * i + 1], lo16, srdO, RFL(so + 4096u));
        }
        asm volatile("s_waitcnt vmcnt(0)\n\ts_barrier" : "+v"(of[0]), "+v"(of[1]), "+v"(of[2]), "+v"(of[3]), "+v"(of[4]), "+v"(of[5]), "+v"(of[6]), "+v"(of[7]) :: "memory");
        asm volatile("" : "+v"(of[8]), "+v"(of[9]), "+v"(of[10]), "+v"(of[11]), "+v"(of[12]), "+v"(of[13]), "+v"(of[14]), "+v"(of[15]) :: "memory");
        STAMP(2);
        f32x4 acc[2][9];
#pragma unroll
        for (int i = 0; i < 2; ++i)
#pragma unroll
            for (int c = 0; c < 9; ++c) acc[i][c] = f32x4{};
#pragma unroll
        for (int jj = 0; jj < 4; ++jj) {
            constexpr unsigned T0[4] = {WSB + 2 * WCH, WSB, WSB + WCH, 0u}, T1[4] = {WCH, 2 * WCH, 81920u, 3 * WCH};
            unsigned a0a = lds0 + (kq ? T1[jj] : T0[jj]) + ab0; asm volatile("" : "+v"(a0a));
            const lds_cptr kb_ = (lds_cptr)(uintptr_t)a0a;
            bf16x8 af_[18];
#pragma unroll
            for (int ks = 0; ks < 2; ++ks)
#pragma unroll
                for (int c = 0; c < 9; ++c) af_[9 * ks + c] = cat8(vtr(kb_ + 9216 * ks + 32 * c), vtr(kb_ + 9216 * ks + 2304 + 32 * c));
            SB();
#pragma unroll
            for (int ks = 0; ks < 2; ++ks)
#pragma unroll
                for (int c = 0; c < 9; ++c) {
                    acc[0][c] = MFMA16(af_[9 * ks + c], of[4 * jj + 2 * ks], acc[0][c]);
                    acc[1][c] = MFMA16(af_[9 * ks + c], of[4 * jj + 2 * ks + 1], acc[1][c]);
                }
            SB();
        }
        LBAR();
        const unsigned xw = (unsigned)w * 9216u + lo16, xr = (unsigned)(w ^ 4) * 9216u + lo16;
#pragma unroll
        for (int c = 0; c < 9; ++c) *(LAS f32x4*)(LW + xw + 1024 * c) = kq ? acc[0][c] : acc[1][c];
        LBAR();
        const unsigned sso = (unsigned)(((((p * 8 + bq + 4) * 8 + s8) * 4 + c4) * 8 + 2 * mg + kq) * 9) * 1024u;
#pragma unroll
        for (int c = 0; c < 9; ++c) { const f32x4 t4 = *(const LAS f32x4*)(LW + xr + 1024 * c); st16_sc1(__builtin_bit_cast(u32x4, (kq ? acc[1][c] : acc[0][c]) + t4), lo16, srdS, RFL(sso + 1024u * c)); }
        WAITBAR(0);
        if (tid == 0) __hip_atomic_fetch_add(cntS(1), 1u, __ATOMIC_RELAXED, __HIP_MEMORY_SCOPE_AGENT);
        STAMP(5);
        wait_cnt(cntS(1), 8u);
        reduce_round(1);
    }
    STAMP(6);
}

extern "C" void kernel_launch(void* const* d_in, const int* in_sizes, int n_in, void* d_out, int out_size, void* d_ws, size_t ws_size, hipStream_t stream) {
    (void)hipFuncSetAttribute((const void*)fused_kernel, hipFuncAttributeMaxDynamicSharedMemorySize, LDS_BYTES);
    (void)hipMemsetAsync((char*)d_ws + WS_CNT, 0, WS_CNT_BYTES, stream);
    hipLaunchKernelGGL(fused_kernel, dim3(256), dim3(512), LDS_BYTES, stream,
                       (const char*)d_in[0], (const char*)d_in[1], (const char*)d_in[2], (const char*)d_in[3], (const char*)d_in[4],
                       (const char*)d_in[5], (const char*)d_in[6], (const char*)d_in[7], (const char*)d_in[8], (const char*)d_in[9],
                       (char*)d_ws, (unsigned short*)d_out);
}
```

```cpp
#include <hip/hip_runtime.h>
#include <hip/hip_bf16.h>
#include <cstdint>

typedef short bf16x8 __attribute__((ext_vector_type(8)));
typedef short s16x4 __attribute__((ext_vector_type(4)));
typedef float f32x16 __attribute__((ext_vector_type(16)));
typedef float f32x4 __attribute__((ext_vector_type(4)));
typedef float f32x2 __attribute__((ext_vector_type(2)));
typedef __bf16 bf16x2_t __attribute__((ext_vector_type(2)));
typedef unsigned u32x4 __attribute__((ext_vector_type(4)));
typedef unsigned u32x2 __attribute__((ext_vector_type(2)));
#define LAS __attribute__((address_space(3)))
#define DI __device__ __forceinline__
typedef LAS const char* lds_cptr;
typedef LAS char* lds_ptr;

constexpr int KVBYTES = 8 * 32 * 1024 * 256;
constexpr unsigned SLOT = 16384;
constexpr int LDS_BYTES = 163840;
constexpr float CSC = 0.08838834764831845f * 1.4426950408889634f;
constexpr float THR = 8.0f;
constexpr unsigned EX = 6 * SLOT, WR = 8 * SLOT;
constexpr unsigned WCH = 18432u, WS0 = 108544u, WS1 = WS0 + WCH, WS2 = WS0 + 2 * WCH, WS3 = WS0 - WCH;
constexpr unsigned WS_E = 0, WS_FO = 4096, WS_FS = 16384;
constexpr size_t WS_O = 65536, WS_O_BYTES = (size_t)2048 * 4096 * 2;
constexpr size_t WS_SL = WS_O + WS_O_BYTES, WS_SL_BYTES = (size_t)16 * 4 * 8 * 4 * 9 * 512;
constexpr unsigned SPIN_CAP = 1u << 20;

#define MFMA32(a, b, c) __builtin_amdgcn_mfma_f32_32x32x16_bf16((a), (b), (c), 0, 0, 0)
#define MFMA16(a, b, c) __builtin_amdgcn_mfma_f32_16x16x32_bf16((a), (b), (c), 0, 0, 0)
#define RFL(x) __builtin_amdgcn_readfirstlane(x)
#define WAITBAR(N) asm volatile("s_waitcnt vmcnt(" #N ") lgkmcnt(0)\n\ts_barrier" ::: "memory")
template <int N> __device__ __forceinline__ void waitbar() { asm volatile("s_waitcnt vmcnt(%0) lgkmcnt(0)\n\ts_barrier" :: "i"(N) : "memory"); }
template <int N> __device__ __forceinline__ void waitv() { asm volatile("s_waitcnt vmcnt(%0)" :: "i"(N) : "memory"); }
#define WB17 WAITBAR(17)
#define WWIN 3
#define XWIN 4
#define LBAR() asm volatile("s_waitcnt lgkmcnt(0)\n\ts_barrier" ::: "memory")
#define SB() __builtin_amdgcn_sched_barrier(0)
#define PIN(x) asm volatile("" : "+v"(x))
#define PIN2(x) asm volatile("" : "+v"(x))

DI unsigned cvtpk(float lo, float hi) { f32x2 v = {lo, hi}; bf16x2_t b = __builtin_convertvector(v, bf16x2_t); return __builtin_bit_cast(unsigned, b); }
DI s16x4 vtr(lds_cptr p) { return __builtin_bit_cast(s16x4, __builtin_amdgcn_ds_read_tr16_b64_v4i16((LAS s16x4*)p)); }
DI bf16x8 cat8(s16x4 a, s16x4 b) { return __builtin_shufflevector(a, b, 0, 1, 2, 3, 4, 5, 6, 7); }
DI bf16x8 ldsrd128(lds_cptr p) { return *(const LAS bf16x8*)p; }
DI float swap32max(float m) { auto rr = __builtin_amdgcn_permlane32_swap(__float_as_uint(m), __float_as_uint(m), false, false); return __builtin_fmaxf(__uint_as_float(rr[0]), __uint_as_float(rr[1])); }
DI float swap32sum(float m) { auto rr = __builtin_amdgcn_permlane32_swap(__float_as_uint(m), __float_as_uint(m), false, false); return __uint_as_float(rr[0]) + __uint_as_float(rr[1]); }
DI bf16x8 pack8s(const f32x16& a, int sg, float sc) {
    u32x4 wv = {cvtpk(a[8 * sg + 0] * sc, a[8 * sg + 1] * sc), cvtpk(a[8 * sg + 2] * sc, a[8 * sg + 3] * sc), cvtpk(a[8 * sg + 4] * sc, a[8 * sg + 5] * sc), cvtpk(a[8 * sg + 6] * sc, a[8 * sg + 7] * sc)};
    return __builtin_bit_cast(bf16x8, wv);
}
#define BC8(x) __builtin_bit_cast(bf16x8, x)
#define BCU(x) __builtin_bit_cast(u32x4, x)

DI void dma16_nt(unsigned ldsdst, unsigned voff, __amdgpu_buffer_rsrc_t srd, unsigned soff) {
    asm volatile("s_mov_b32 m0, %0\n\ts_nop 0\n\tbuffer_load_dwordx4 %1, %2, %3 offen nt lds" :: "s"(ldsdst), "v"(voff), "s"(srd), "s"(soff) : "memory");
}
DI void dma16(unsigned ldsdst, unsigned voff, __amdgpu_buffer_rsrc_t srd, unsigned soff) {
    asm volatile("s_mov_b32 m0, %0\n\ts_nop 0\n\tbuffer_load_dwordx4 %1, %2, %3 offen lds" :: "s"(ldsdst), "v"(voff), "s"(srd), "s"(soff) : "memory");
}
DI void st16_sc1(u32x4 v, unsigned voff, __amdgpu_buffer_rsrc_t srd) { __builtin_amdgcn_raw_buffer_store_b128(v, srd, (int)voff, 0, 16); }
DI void st8_sc1(u32x2 v, unsigned voff, __amdgpu_buffer_rsrc_t srd) { __builtin_amdgcn_raw_buffer_store_b64(v, srd, (int)voff, 0, 16); }
DI void st4_sc1(unsigned v, unsigned voff, __amdgpu_buffer_rsrc_t srd) { __builtin_amdgcn_raw_buffer_store_b32(v, srd, (int)voff, 0, 16); }
#define LD16_SC1(dst, voff, srd, soff) asm volatile("buffer_load_dwordx4 %0, %1, %2, %3 offen sc1" : "=v"(dst) : "v"(voff), "s"(srd), "s"(soff) : "memory")
#define LD8_SC1(dst, voff, srd, soff) asm volatile("buffer_load_dwordx2 %0, %1, %2, %3 offen sc1" : "=v"(dst) : "v"(voff), "s"(srd), "s"(soff) : "memory")
#define LD4_SC1(dst, voff, srd) asm volatile("buffer_load_dword %0, %1, %2, 0 offen sc1" : "=v"(dst) : "v"(voff), "s"(srd) : "memory")
#define LD16(dst, voff, srd, soff) asm volatile("buffer_load_dwordx4 %0, %1, %2, %3 offen" : "=v"(dst) : "v"(voff), "s"(srd), "s"(soff) : "memory")
DI void wait_tag(unsigned voff, __amdgpu_buffer_rsrc_t srd, unsigned tag) {
    for (unsigned sp = 0; sp < SPIN_CAP; ++sp) {
        unsigned f;
        asm volatile("buffer_load_dword %0, %1, %2, 0 offen sc1\n\ts_waitcnt vmcnt(0)" : "=v"(f) : "v"(voff), "s"(srd) : "memory");
        if (__all(f == tag)) break;
        __builtin_amdgcn_s_sleep(1);
    }
}

#define STAMP(i)
#define STAMPT(T)

__global__ __launch_bounds__(512, 2) void fused_kernel(const char* __restrict__ xA, const char* __restrict__ kA, const char* __restrict__ vA, const char* __restrict__ wqA, const char* __restrict__ woA,
                                                      const char* __restrict__ xB, const char* __restrict__ kB, const char* __restrict__ vB, const char* __restrict__ wqB, const char* __restrict__ woB,
                                                      char* __restrict__ ws, unsigned short* __restrict__ out) {
    extern __shared__ __attribute__((aligned(16))) char lds[];
    const int tid = threadIdx.x, lane = tid & 63;
    const int w = RFL(tid >> 6);
    const int r32 = lane & 31, hi = lane >> 5;
    const int wg = blockIdx.x, xcd = wg & 7, idx = wg >> 3;
    const int p = xcd & 1, hq = xcd >> 1, c4 = idx & 3, b = idx >> 2;
    const int g = p * 8 + b;
    const int h0 = 8 * hq + 2 * c4;
    const unsigned lds0 = (unsigned)(uintptr_t)lds;
    const lds_cptr L = (lds_cptr)lds;
    const lds_ptr LW = (lds_ptr)lds;
    STAMP(0);
    const __amdgpu_buffer_rsrc_t srdX = __builtin_amdgcn_make_buffer_rsrc((void*)(p ? xB : xA), (short)0, 8 * 128 * 576 * 2, 0x00020000);
    const __amdgpu_buffer_rsrc_t srdW = __builtin_amdgcn_make_buffer_rsrc((void*)(p ? wqB : wqA), (short)0, 576 * 4096 * 2, 0x00020000);
    const __amdgpu_buffer_rsrc_t srdK = __builtin_amdgcn_make_buffer_rsrc((void*)(p ? kB : kA), (short)0, KVBYTES, 0x00020000);
    const __amdgpu_buffer_rsrc_t srdV = __builtin_amdgcn_make_buffer_rsrc((void*)(p ? vB : vA), (short)0, KVBYTES, 0x00020000);
    const __amdgpu_buffer_rsrc_t srdWo = __builtin_amdgcn_make_buffer_rsrc((void*)(p ? woB : woA), (short)0, 4096 * 576 * 2, 0x00020000);
    const __amdgpu_buffer_rsrc_t srdO = __builtin_amdgcn_make_buffer_rsrc((void*)(ws + WS_O), (short)0, (int)WS_O_BYTES, 0x00020000);
    const __amdgpu_buffer_rsrc_t srdS = __builtin_amdgcn_make_buffer_rsrc((void*)(ws + WS_SL), (short)0, (int)WS_SL_BYTES, 0x00020000);
    const __amdgpu_buffer_rsrc_t srdF = __builtin_amdgcn_make_buffer_rsrc((void*)ws, (short)0, 65536, 0x00020000);

    const bool isB = w >= 4;
    const int bw = w & 3;
    const int r7 = (lane >> 2) & 7;
    const int blk = (lane >> 4) & 1, q4 = (lane & 15) >> 2, p4 = lane & 3, g4 = lane >> 4;
    const unsigned lo16 = (unsigned)lane * 16u;
    unsigned ev;
#define ELOAD() do { const unsigned eo_ = WS_E + 64u * (unsigned)g; asm volatile("buffer_load_dword %0, %1, %2, 0 offen sc1" : "=v"(ev) : "v"(eo_), "s"(srdF) : "memory"); } while (0)
#define ETAG() do { PIN(ev); tag = (unsigned)RFL(ev) * 0x9E3779B1u + 0x7F4A7C15u; } while (0)
    const unsigned chs0 = 16u * (4u * (unsigned)hi + (unsigned)((lane & 3) ^ ((2 * (2 * bw) + (r7 >> 2)) & 3)));
    const unsigned chs1 = 16u * (4u * (unsigned)hi + (unsigned)((lane & 3) ^ ((2 * (2 * bw + 1) + (r7 >> 2)) & 3)));
    const unsigned srcKV0 = (unsigned)(16 * bw + r7) * 256u + chs0, srcKV1 = (unsigned)(16 * bw + 8 + r7) * 256u + chs1;
    const unsigned srcW0 = (unsigned)(16 * bw + r7) * 8192u + chs0, srcW1 = (unsigned)(16 * bw + 8 + r7) * 8192u + chs1;
    auto kv_off = [&](int T) -> unsigned { return (unsigned)((b * 32 + h0) * 1024 + 64 * T) * 256u; };
    auto kslot = [&](int T) -> unsigned { return (unsigned)(T % 3) * SLOT; };
    auto vslot = [&](int T) -> unsigned { return (unsigned)(3 + T % 3) * SLOT; };
    auto dma_tile = [&](__amdgpu_buffer_rsrc_t srd, int T, unsigned slot) { const unsigned so = kv_off(T), d = lds0 + 4096u * (unsigned)bw + slot;
        dma16_nt(RFL(d), srcKV0, srd, RFL(so)); dma16_nt(RFL(d + 1024), srcKV0, srd, RFL(so + 128));
        dma16_nt(RFL(d + 2048), srcKV1, srd, RFL(so)); dma16_nt(RFL(d + 3072), srcKV1, srd, RFL(so + 128)); };
    auto dma_k = [&](int T) { dma_tile(srdK, T, kslot(T)); };
    auto dma_v = [&](int T) { dma_tile(srdV, T, vslot(T)); };
    const int rot = (b + 2 * c4) % 9;
    auto dma_wjob = [&](int head, int n, unsigned dst) {
        const int ps = n / 9, kc0 = n - 9 * ps + rot, kc = kc0 >= 9 ? kc0 - 9 : kc0;
        const unsigned sw = (unsigned)(64 * kc) * 8192u + (unsigned)head * 256u + 128u * (unsigned)ps, d = lds0 + dst + 2048u * (unsigned)bw;
        dma16(RFL(d), srcW0, srdW, RFL(sw)); dma16(RFL(d + 1024), srcW1, srdW, RFL(sw));
    };
    auto dma_wdj = [&](int head, int gj, unsigned dst) { dma_wjob(head, 2 * gj, dst); dma_wjob(head, 2 * gj + 1, dst + 8192u); };
    unsigned tag;

    f32x4 oacc[2][9];
    if (isB) {
        bf16x8 xf[36];
        STAMP(41);
        const unsigned vx = (unsigned)(32 * bw + (lane >> 3)) * 1152u + 16u * (lane & 7);
        const unsigned sx = (unsigned)(b * 128) * 1152u;
#define XLOAD(c) do { const int kk_ = (c) + rot; const unsigned so_ = sx + 128u * (unsigned)(kk_ >= 9 ? kk_ - 9 : kk_); \
            LD16(xf[4 * (c)], vx, srdX, RFL(so_)); LD16(xf[4 * (c) + 1], vx, srdX, RFL(so_ + 9216u)); LD16(xf[4 * (c) + 2], vx, srdX, RFL(so_ + 18432u)); LD16(xf[4 * (c) + 3], vx, srdX, RFL(so_ + 27648u)); } while (0)
        XLOAD(0); XLOAD(1); XLOAD(2); XLOAD(3);
        if (XWIN > 4) XLOAD(4); if (XWIN > 5) XLOAD(5); if (XWIN > 6) XLOAD(6); if (XWIN > 7) XLOAD(7); if (XWIN > 8) XLOAD(8);
        const unsigned xwe = lds0 + EX + 4096u * bw + (unsigned)(lane >> 3) * 128u + 16u * (unsigned)((lane & 7) ^ ((lane >> 4) & 7));
        const unsigned xwo = lds0 + EX + 4096u * bw + (unsigned)(8 + (lane >> 3)) * 128u + 16u * (unsigned)((lane & 7) ^ ((4 + (lane >> 4)) & 7));
        const unsigned xrd = lds0 + EX + 4096u * bw + (unsigned)r32 * 128u + 16u * (unsigned)(hi ^ ((r32 >> 1) & 7));
        const unsigned wb0 = lds0 + 1024u * hi + 64u * (0 + q4) + 16u * ((2 * blk + (p4 & 1)) ^ (2 * hi + 0)) + 8u * (p4 >> 1);
        const unsigned wb1 = lds0 + 1024u * hi + 64u * (4 + q4) + 16u * ((2 * blk + (p4 & 1)) ^ (2 * hi + 1)) + 8u * (p4 >> 1);
        const unsigned qex = EX + 4096u * bw + lo16;
        f32x16 acc[2];
        bf16x8 ar[8];
#define XCONV(c) do { asm volatile("s_waitcnt vmcnt(%4)" : "+v"(xf[4 * (c)]), "+v"(xf[4 * (c) + 1]), "+v"(xf[4 * (c) + 2]), "+v"(xf[4 * (c) + 3]) : "i"(4 * ((((c) - 1 + XWIN < 8) ? (c) - 1 + XWIN : 8) - (c))) : "memory"); \
            *(LAS bf16x8*)(uintptr_t)(xwe) = xf[4 * (c)]; *(LAS bf16x8*)(uintptr_t)(xwo) = xf[4 * (c) + 1]; *(LAS bf16x8*)(uintptr_t)(xwe + 2048) = xf[4 * (c) + 2]; *(LAS bf16x8*)(uintptr_t)(xwo + 2048) = xf[4 * (c) + 3]; \
            asm volatile("" ::: "memory"); \
            _Pragma("unroll") for (int ks = 0; ks < 4; ++ks) xf[4 * (c) + ks] = ldsrd128((lds_cptr)(uintptr_t)(xrd ^ (32u * ks))); \
            asm volatile("" ::: "memory"); \
            if ((c) + XWIN <= 8) XLOAD(((c) + XWIN <= 8) ? (c) + XWIN : 0); } while (0)
#define WFR(wp0, wp1, m) cat8(vtr((wp0) + 8192 * ((m) >> 3) + 2048 * (((m) >> 1) & 3) + 512 * ((m) & 1)), vtr((wp1) + 8192 * ((m) >> 3) + 2048 * (((m) >> 1) & 3) + 512 * ((m) & 1)))
#define WPTR(p0, p1, base) unsigned p0##a = wb0 + (base), p1##a = wb1 + (base); asm volatile("" : "+v"(p0##a), "+v"(p1##a)); const lds_cptr p0 = (lds_cptr)(uintptr_t)p0##a, p1 = (lds_cptr)(uintptr_t)p1##a
#define QMF(gj, m) do { constexpr int n_ = 2 * (gj) + ((m) >> 3), kc_ = n_ % 9, ks_ = ((m) >> 1) & 3, dl_ = (m) & 1; \
            if (kc_ == 0 && ks_ == 0) acc[dl_] = MFMA32(ar[(m) & 7], xf[4 * kc_ + ks_], (f32x16{})); else acc[dl_] = MFMA32(ar[(m) & 7], xf[4 * kc_ + ks_], acc[dl_]); \
            PIN(acc[dl_]); \
            if (kc_ == 8 && ((m) & 7) == 7) { _Pragma("unroll") for (int dl = 0; dl < 2; ++dl) _Pragma("unroll") for (int sg = 0; sg < 2; ++sg) \
                *(LAS bf16x8*)(LW + qex + 16384u * (n_ / 9) + (2 * dl + sg) * 1024) = pack8s(acc[dl], sg, CSC); } } while (0)
#define QSTREAM(gj, base, MID, HASNEXT, nbase) do { \
            { WPTR(c0, c1, base); \
              QMF(gj, 0); ar[6] = WFR(c0, c1, 6); SB(); QMF(gj, 1); ar[7] = WFR(c0, c1, 7); SB(); QMF(gj, 2); ar[0] = WFR(c0, c1, 8); SB(); QMF(gj, 3); ar[1] = WFR(c0, c1, 9); SB(); \
              QMF(gj, 4); ar[2] = WFR(c0, c1, 10); SB(); QMF(gj, 5); ar[3] = WFR(c0, c1, 11); SB(); QMF(gj, 6); ar[4] = WFR(c0, c1, 12); SB(); QMF(gj, 7); ar[5] = WFR(c0, c1, 13); SB(); \
              QMF(gj, 8); ar[6] = WFR(c0, c1, 14); SB(); QMF(gj, 9); ar[7] = WFR(c0, c1, 15); SB(); QMF(gj, 10); SB(); QMF(gj, 11); SB(); } \
            MID; SB(); \
            if (HASNEXT) { WPTR(n0, n1, nbase); \
              QMF(gj, 12); ar[0] = WFR(n0, n1, 0); ar[1] = WFR(n0, n1, 1); SB(); QMF(gj, 13); ar[2] = WFR(n0, n1, 2); ar[3] = WFR(n0, n1, 3); SB(); \
              QMF(gj, 14); SB(); QMF(gj, 15); ar[4] = WFR(n0, n1, 4); ar[5] = WFR(n0, n1, 5); SB(); } \
            else { QMF(gj, 12); SB(); QMF(gj, 13); SB(); QMF(gj, 14); SB(); QMF(gj, 15); SB(); } \
        } while (0)
#define QDJ(T, gj, base) do { STAMPT(T); WPTR(c0, c1, base); \
            dma_k((T) + 2); \
            _Pragma("unroll") for (int m = 0; m < 8; ++m) ar[m] = WFR(c0, c1, m); SB(); \
            QMF(gj, 0); QMF(gj, 1); QMF(gj, 2); QMF(gj, 3); QMF(gj, 4); QMF(gj, 5); QMF(gj, 6); QMF(gj, 7); SB(); \
            dma_v((T) + 1); \
            _Pragma("unroll") for (int m = 8; m < 16; ++m) ar[m & 7] = WFR(c0, c1, m); SB(); \
            QMF(gj, 8); QMF(gj, 9); QMF(gj, 10); QMF(gj, 11); QMF(gj, 12); QMF(gj, 13); QMF(gj, 14); QMF(gj, 15); SB(); } while (0)
        STAMP(1);
        XCONV(0); STAMP(42); XCONV(1);
        LBAR(); STAMP(43);
        { WPTR(c0, c1, 0u); ar[0] = WFR(c0, c1, 0); ar[1] = WFR(c0, c1, 1); ar[2] = WFR(c0, c1, 2); ar[3] = WFR(c0, c1, 3); ar[4] = WFR(c0, c1, 4); ar[5] = WFR(c0, c1, 5); }
        STAMP(44); QSTREAM(0, 0u, LBAR(), true, SLOT);     XCONV(2); XCONV(3); STAMP(45);
        QSTREAM(1, SLOT, LBAR(), true, 3 * SLOT);          XCONV(4); XCONV(5);
        QSTREAM(2, 3 * SLOT, LBAR(), true, 2 * SLOT);      XCONV(6); XCONV(7);
        QSTREAM(3, 2 * SLOT, LBAR(), true, 4 * SLOT);      XCONV(8);
        STAMP(46); QSTREAM(4, 4 * SLOT, LBAR(), true, 5 * SLOT); STAMP(47);
        QSTREAM(5, 5 * SLOT, LBAR(), true, EX + SLOT);
        QSTREAM(6, EX + SLOT, LBAR(), true, WR);
        QSTREAM(7, WR, LBAR(), true, WR + SLOT);
        QSTREAM(8, WR + SLOT, {}, false, 0u);
        STAMP(3);
        LBAR();
#define BKV(T) do { STAMPT(T); if ((T) < 30) dma_k((T) + 2); if ((T) < 31) dma_v((T) + 1); } while (0)
        BKV(0); dma_wdj(h0 + 1, 0, WR); dma_wdj(h0 + 1, 1, WR + SLOT); WAITBAR(16);
        BKV(1); ELOAD(); WAITBAR(9);
        QDJ(2, 0, WR); WAITBAR(8);
        QDJ(3, 1, WR + SLOT); ETAG(); dma_wdj(h0 + 1, 2, WR); WAITBAR(12);
        BKV(4); dma_wdj(h0 + 1, 3, WR + SLOT); WAITBAR(12);
        QDJ(5, 2, WR); WAITBAR(8);
        QDJ(6, 3, WR + SLOT); dma_wdj(h0 + 1, 4, WR); WAITBAR(12);
        BKV(7); dma_wdj(h0 + 1, 5, WR + SLOT); WAITBAR(12);
        QDJ(8, 4, WR); WAITBAR(8);
        QDJ(9, 5, WR + SLOT); dma_wdj(h0 + 1, 6, WR); WAITBAR(12);
        BKV(10); dma_wdj(h0 + 1, 7, WR + SLOT); WAITBAR(12);
        QDJ(11, 6, WR); WAITBAR(8);
        QDJ(12, 7, WR + SLOT); dma_wdj(h0 + 1, 8, WR); WAITBAR(12);
        BKV(13); WAITBAR(8);
        QDJ(14, 8, WR); WAITBAR(8);
        BKV(15); WAITBAR(8);
        {
            int ln = lane; asm volatile("" : "+v"(ln));
            unsigned wov[5];
#pragma unroll
            for (int i = 0; i < 5; ++i) {
                const int pc = (i < 4) ? bw + 4 * i : 16 + (bw & 1);
                const int uu = 64 * pc + ln, pos = uu / 18, ch = uu - 18 * pos;
                const int k = (pos & ~12) | ((pos & 4) << 1) | ((pos & 8) >> 1);
                wov[i] = (unsigned)k * 1152u + 16u * ch;
            }
            const unsigned ab0 = lds0 + (unsigned)(16 * ((ln >> 4) >> 1) + 4 * ((ln >> 4) & 1) + ((ln & 15) >> 2)) * 288u + 8u * (ln & 3);
            auto dma_wo = [&](int r, int jj, unsigned dst) {
                const int hd = 8 * hq + 2 * ((c4 + (jj >> 1)) & 3) + r;
                const unsigned so = (unsigned)(128 * hd + 64 * (jj & 1)) * 1152u + 288u * c4;
#pragma unroll
                for (int i = 0; i < 5; ++i) { const int pc = (i < 4) ? bw + 4 * i : 16 + (bw & 1); dma16(RFL(lds0 + dst + 1024u * pc), wov[i], srdWo, RFL(so)); }
            };
#pragma unroll
            for (int i = 0; i < 2; ++i)
#pragma unroll
                for (int c = 0; c < 9; ++c) oacc[i][c] = f32x4{};
            bf16x8 ofx[4], ofy[4];
            unsigned flagv;
            const unsigned fo1 = WS_FO + (unsigned)((((g * 4 + hq) * 2 + 0) * 4 + bw) * 4 + (ln & 3)) * 4u;
#define O1L(OF, jj) do { const int hd_ = 8 * hq + 2 * ((c4 + ((jj) >> 1)) & 3); const unsigned so_ = (unsigned)((((g * 32 + hd_) * 8 + 2 * bw) * 4) + 2 * ((jj) & 1)) * 1024u; \
                LD16_SC1(OF[0], lo16, srdO, RFL(so_)); LD16_SC1(OF[1], lo16, srdO, RFL(so_ + 4096u)); LD16_SC1(OF[2], lo16, srdO, RFL(so_ + 1024u)); LD16_SC1(OF[3], lo16, srdO, RFL(so_ + 5120u)); } while (0)
#define OPHALF(base, OF, ks) do { \
                unsigned a0a = ab0 + (base) + 9216u * (ks); asm volatile("" : "+v"(a0a)); \
                const lds_cptr kb_ = (lds_cptr)(uintptr_t)a0a; bf16x8 af_[9]; \
                _Pragma("unroll") for (int c = 0; c < 9; ++c) af_[c] = cat8(vtr(kb_ + 32 * c), vtr(kb_ + 2304 + 32 * c)); \
                SB(); \
                _Pragma("unroll") for (int c = 0; c < 9; ++c) { oacc[0][c] = MFMA16(af_[c], OF[2 * (ks)], oacc[0][c]); oacc[1][c] = MFMA16(af_[c], OF[2 * (ks) + 1], oacc[1][c]); PIN(oacc[0][c]); PIN(oacc[1][c]); } \
                SB(); \
            } while (0)
#define WSL(jj) ((jj) % 3 == 0 ? WS2 : (jj) % 3 == 1 ? WS0 : WS1)
#define ISSUE1(jj, OF) do { dma_wo(0, jj, WSL(jj)); O1L(OF, jj); } while (0)
#define BKVC(T, jj, OF) do { STAMPT(T); asm volatile("" : "+v"(OF[0]), "+v"(OF[1]), "+v"(OF[2]), "+v"(OF[3]) :: "memory"); \
                if ((T) < 30) dma_k((T) + 2); OPHALF(WSL(jj), OF, 0); if ((T) < 31) dma_v((T) + 1); OPHALF(WSL(jj), OF, 1); } while (0)
            BKV(16); WAITBAR(8);
            {
                unsigned oa = lds0 + EX + 4096u * bw + lo16; asm volatile("" : "+v"(oa)); const lds_cptr op = (lds_cptr)(uintptr_t)oa;
                ofx[0] = ldsrd128(op); ofx[1] = ldsrd128(op + 16384); ofx[2] = ldsrd128(op + 1024); ofx[3] = ldsrd128(op + 17408);
                ofy[0] = ldsrd128(op + 2048); ofy[1] = ldsrd128(op + 18432); ofy[2] = ldsrd128(op + 3072); ofy[3] = ldsrd128(op + 19456); }
            BKV(17); dma_wo(0, 0, WSL(0));
            {   const unsigned so_ = (unsigned)((((g * 32 + h0) * 8 + 2 * bw) * 4)) * 1024u + lo16;
#pragma unroll
                for (int i = 0; i < 4; ++i) { st16_sc1(BCU(ofx[i]), so_ + 4096u * (i & 1) + 1024u * (i >> 1), srdO); st16_sc1(BCU(ofy[i]), so_ + 4096u * (i & 1) + 1024u * (2 + (i >> 1)), srdO); } }
            WAITBAR(21);
            BKV(18); dma_wo(0, 1, WSL(1)); WAITBAR(13);
            if (ln == 0) st4_sc1(tag, WS_FO + (unsigned)((((g * 4 + hq) * 2 + 0) * 4 + bw) * 4 + c4) * 4u, srdF);
            BKVC(19, 0, ofx); WAITBAR(8);
            BKVC(20, 1, ofy); WAITBAR(8);
            LD4_SC1(flagv, fo1, srdF);
            BKV(21); WAITBAR(8);
            BKV(22);
            asm volatile("" : "+v"(flagv) :: "memory");
            if (!__all(flagv == tag)) wait_tag(fo1, srdF, tag);
            ISSUE1(2, ofx); WB17;
            BKV(23); ISSUE1(3, ofy); WB17;
            BKVC(24, 2, ofx); ISSUE1(4, ofx); WB17;
            BKVC(25, 3, ofy); ISSUE1(5, ofy); WB17;
            BKVC(26, 4, ofx); ISSUE1(6, ofx); WB17;
            BKVC(27, 5, ofy); ISSUE1(7, ofy); WB17;
            BKVC(28, 6, ofx); dma_wo(1, 0, WS1); WAITBAR(13);
            BKVC(29, 7, ofy); dma_wo(1, 1, WS2); WAITBAR(13);
            BKV(30); dma_wo(1, 2, WS0); WAITBAR(9);
            BKV(31); dma_wo(1, 3, WS3); WAITBAR(5);
            STAMPT(32);
            dma_wo(1, 4, 0u); dma_wo(1, 5, WCH); dma_wo(1, 6, 2u * WCH);
            WAITBAR(15);
            dma_wo(1, 7, 3u * WCH);
        }
    } else {
        {
            constexpr unsigned PS[9] = {0u, SLOT, 3 * SLOT, 2 * SLOT, 4 * SLOT, 5 * SLOT, EX + SLOT, WR, WR + SLOT};
#pragma unroll
            for (int s = 0; s < WWIN; ++s) dma_wdj(h0, s, PS[s]);
            STAMP(41);
#define PSTEP(s) do { waitbar<4 * (((s) - 1 + WWIN < 8 ? (s) - 1 + WWIN : 8) - (s)) + ((s) == 7 ? 4 : (s) == 8 ? 12 : 0)>(); \
                if ((s) + WWIN <= 8) dma_wdj(h0, ((s) + WWIN <= 8) ? (s) + WWIN : 0, PS[((s) + WWIN <= 8) ? (s) + WWIN : 0]); } while (0)
            PSTEP(0); STAMP(42); PSTEP(1); PSTEP(2); PSTEP(3); PSTEP(4); PSTEP(5);
            PSTEP(6); dma_k(0);
            PSTEP(7); dma_k(1); dma_v(0);
            PSTEP(8);
            WAITBAR(8);
            ELOAD();
        }
        const int qb = w;
        bf16x8 qf[8];
        { unsigned qa = lds0 + EX + 4096u * qb + lo16; asm volatile("" : "+v"(qa));
#pragma unroll
          for (int s = 0; s < 8; ++s) qf[s] = ldsrd128((lds_cptr)(uintptr_t)qa + 16384 * (s >> 2) + 1024 * (s & 3)); }
        const unsigned kb0 = 2048u * (r32 >> 3) + 64u * (r32 & 7) + 16u * ((0 + hi) ^ ((r32 >> 2) & 3));
        const unsigned kb1 = 2048u * (r32 >> 3) + 64u * (r32 & 7) + 16u * ((2 + hi) ^ ((r32 >> 2) & 3));
        const unsigned vb0 = 64u * (4 * hi + q4) + 16u * ((2 * blk + (p4 & 1)) ^ (0 + hi)) + 8u * (p4 >> 1);
        const unsigned vb1 = 2048u + 64u * (4 * hi + q4) + 16u * ((2 * blk + (p4 & 1)) ^ (2 + hi)) + 8u * (p4 >> 1);
        f32x16 o[4];
#pragma unroll
        for (int i = 0; i < 4; ++i) o[i] = f32x16{};
        f32x2 lsum = {0.f, 0.f};
        float fpend = 1.f; bool pend = false;
        f32x16 sA = f32x16{}, sB = f32x16{}, negm = f32x16{};
        u32x4 pA0 = {}, pA1 = {}, pB0 = {}, pB1 = {};
        bf16x8 vf0 = {}, vf1 = {}, vf2 = {}, vf3 = {};
        const unsigned kbb0 = lds0 + kb0, kbb1 = lds0 + kb1, vbb0 = lds0 + vb0 + 3 * SLOT, vbb1 = lds0 + vb1 + 3 * SLOT;
        unsigned scur = 0, sprev = 2 * SLOT;
#define KRD(kp, ks) ldsrd128((((ks) & 1) ? k1p : k0p) + (kp) + 512 * ((ks) >> 1))
#define VRDX(a0, a1, vp, sg, db) cat8(vtr((a0) + (vp) + 4096 * (sg) + 512 * (db)), vtr((a1) + (vp) + 4096 * (sg) + 512 * (db)))
#define VRD(vp, sg, db) VRDX(v0p, v1p, vp, sg, db)
#define BASES() asm volatile("" : "+s"(scur), "+s"(sprev)); \
            unsigned k0a = kbb0 + scur, k1a = kbb1 + scur, v0a = vbb0 + sprev, v1a = vbb1 + sprev, n0a = vbb0 + scur, n1a = vbb1 + scur; \
            asm volatile("" : "+v"(k0a), "+v"(k1a), "+v"(v0a), "+v"(v1a), "+v"(n0a), "+v"(n1a)); \
            const lds_cptr k0p = (lds_cptr)(uintptr_t)k0a, k1p = (lds_cptr)(uintptr_t)k1a, v0p = (lds_cptr)(uintptr_t)v0a, v1p = (lds_cptr)(uintptr_t)v1a, n0p = (lds_cptr)(uintptr_t)n0a, n1p = (lds_cptr)(uintptr_t)n1a
#define ADVANCE() do { sprev = scur; scur = (scur == 2 * SLOT) ? 0u : scur + SLOT; } while (0)
#define EX1(S, i) do { S[i] = __builtin_amdgcn_exp2f(S[i]); } while (0)
#define MX3(a, b, c) __builtin_fmaxf(__builtin_fmaxf(a, b), c)
#define AMAX3(d, a, b, c) asm volatile("v_max3_f32 %0, %1, %2, %3" : "=v"(d) : "v"(a), "v"(b), "v"(c))
#define AMAX2(d, a, b) asm volatile("v_max_f32 %0, %1, %2" : "=v"(d) : "v"(a), "v"(b))
#define PAIR(S, i) (f32x2{S[2 * (i)], S[2 * (i) + 1]})
#define RMAX1(S) ma_ = MX3(S[0], S[1], S[2]); mb_ = MX3(S[3], S[4], S[5]); mc_ = MX3(S[6], S[7], S[8]); md_ = MX3(S[9], S[10], S[11]); me_ = MX3(S[12], S[13], S[14]); \
            ma_ = MX3(ma_, mb_, mc_); md_ = MX3(md_, me_, S[15]); ma_ = MX3(ma_, md_, 0.f); PIN(ma_)
#define RMAX2() do { auto rr_ = __builtin_amdgcn_permlane32_swap(__float_as_uint(ma_), __float_as_uint(ma_), false, false); \
            rm_ = MX3(__uint_as_float(rr_[0]), __uint_as_float(rr_[1]), 0.f); PIN(rm_); } while (0)
#define APPLY_PEND() do { if (__builtin_expect(pend, 0)) { _Pragma("unroll") for (int d = 0; d < 4; ++d) _Pragma("unroll") for (int i = 0; i < 16; ++i) o[d][i] *= fpend; pend = false; } } while (0)
#define DECIDE(SN, PP1, DOPV) do { \
            if (__builtin_expect(__any(rm_ > THR), 0)) { const float dl_ = rm_; \
                _Pragma("unroll") for (int i = 0; i < 16; ++i) { SN[i] -= dl_; negm[i] -= dl_; } \
                fpend = __builtin_amdgcn_exp2f(-dl_); lsum *= fpend; pend = true; } \
        } while (0)
#define SLOTM(SC, SN, PP0, PP1, PC0, PC1, kp, vp, DOQK, DOPV, MID) do { \
            float rm_, ma_, mb_, mc_, md_, me_; f32x2 s0_, s1_; \
            bf16x8 k0_, k1_, k2_, k3_; \
            if (DOQK) { k1_ = KRD(kp, 1); k0_ = KRD(kp, 0); k3_ = KRD(kp, 3); k2_ = KRD(kp, 2); } \
            SB(); \
            APPLY_PEND(); \
              if (DOPV) o[0] = MFMA32(vf0, BC8(PP0), o[0]); RMAX1(SC); SB(); \
              if (DOPV) { o[1] = MFMA32(vf1, BC8(PP0), o[1]); vf1 = VRD(vp, 1, 1); vf0 = VRD(vp, 1, 0); } RMAX2(); SB(); \
              if (DOPV) o[2] = MFMA32(vf2, BC8(PP0), o[2]); SB(); \
              if (DOPV) { o[3] = MFMA32(vf3, BC8(PP0), o[3]); vf3 = VRD(vp, 1, 3); vf2 = VRD(vp, 1, 2); } SB(); \
            DECIDE(SC, PP1, DOPV); SB(); \
              if (DOQK) SN = MFMA32(k0_, qf[0], negm); EX1(SC, 0); EX1(SC, 1); PIN(SC); SB(); \
              if (DOPV) o[0] = MFMA32(vf0, BC8(PP1), o[0]); EX1(SC, 2); EX1(SC, 3); PC0[0] = cvtpk(SC[0], SC[1]); PIN(SC); PIN(PC0); SB(); \
              if (DOQK) { SN = MFMA32(k1_, qf[1], SN); k1_ = KRD(kp, 5); k0_ = KRD(kp, 4); } s0_ = PAIR(SC, 0) + PAIR(SC, 1); PIN2(s0_); EX1(SC, 4); EX1(SC, 5); PIN(SC); SB(); \
              if (DOPV) o[1] = MFMA32(vf1, BC8(PP1), o[1]); PC0[1] = cvtpk(SC[2], SC[3]); EX1(SC, 6); PC0[2] = cvtpk(SC[4], SC[5]); PIN(SC); PIN(PC0); SB(); \
              if (DOQK) SN = MFMA32(k2_, qf[2], SN); s0_ += PAIR(SC, 2); PIN2(s0_); EX1(SC, 7); EX1(SC, 8); PIN(SC); SB(); \
              if (DOPV) o[2] = MFMA32(vf2, BC8(PP1), o[2]); EX1(SC, 9); PC0[3] = cvtpk(SC[6], SC[7]); PIN(SC); PIN(PC0); SB(); \
              if (DOQK) { SN = MFMA32(k3_, qf[3], SN); k3_ = KRD(kp, 7); k2_ = KRD(kp, 6); } s1_ = PAIR(SC, 3) + PAIR(SC, 4); PIN2(s1_); EX1(SC, 10); EX1(SC, 11); PIN(SC); SB(); \
              if (DOPV) o[3] = MFMA32(vf3, BC8(PP1), o[3]); PC1[0] = cvtpk(SC[8], SC[9]); EX1(SC, 12); PC1[1] = cvtpk(SC[10], SC[11]); PIN(SC); PIN(PC1); SB(); \
              if (DOQK) SN = MFMA32(k0_, qf[4], SN); s1_ += PAIR(SC, 5); PIN2(s1_); EX1(SC, 13); EX1(SC, 14); PIN(SC); SB(); \
              if (DOQK) SN = MFMA32(k1_, qf[5], SN); EX1(SC, 15); PC1[2] = cvtpk(SC[12], SC[13]); PIN(SC); PIN(PC1); SB(); \
            MID; SB(); \
              if (DOQK) SN = MFMA32(k2_, qf[6], SN); s0_ += PAIR(SC, 6); s1_ += PAIR(SC, 7); PIN2(s0_); PIN2(s1_); PC1[3] = cvtpk(SC[14], SC[15]); PIN(PC1); SB(); \
              if (DOQK) SN = MFMA32(k3_, qf[7], SN); lsum += s0_; lsum += s1_; PIN2(lsum); SB(); \
        } while (0)
#define VPRE(a0, a1, vp) do { vf1 = VRDX(a0, a1, vp, 0, 1); vf0 = VRDX(a0, a1, vp, 0, 0); vf3 = VRDX(a0, a1, vp, 0, 3); vf2 = VRDX(a0, a1, vp, 0, 2); } while (0)
#define WT0()
#define WT1(Tn)
#define ABAR(Tn) do { WT0(); LBAR(); WT1(Tn); STAMPT(Tn); } while (0)
        STAMP(8);
#pragma unroll 1
        for (int r = 0; r < 2; ++r) {
            {
                BASES();
#pragma unroll
                for (int ks = 0; ks < 8; ++ks) sA = MFMA32(KRD(0, ks), qf[ks], ks ? sA : (f32x16{}));
                float rm; { float a_ = MX3(sA[0], sA[1], sA[2]), b_ = MX3(sA[3], sA[4], sA[5]); a_ = MX3(a_, sA[6], sA[7]); b_ = MX3(b_, sA[8], sA[9]); a_ = MX3(a_, sA[10], sA[11]); b_ = MX3(b_, sA[12], sA[13]);
                    a_ = MX3(a_, sA[14], sA[15]); rm = swap32max(__builtin_fmaxf(a_, b_)); }
#pragma unroll
                for (int i = 0; i < 16; ++i) { sA[i] -= rm; negm[i] = -rm; }
                pend = false;
                SLOTM(sA, sB, pB0, pB1, pA0, pA1, 8192, 0, true, false, { if (r == 0) asm volatile("s_waitcnt vmcnt(0)" ::: "memory");
                    ABAR(16 * r + 1); VPRE(n0p, n1p, 0); });
                ADVANCE();
            }
#pragma unroll 1
            for (int t = 1; t < 16; ++t) {
                BASES();
                SLOTM(sB, sA, pA0, pA1, pB0, pB1, 0, 0, true, true, { VPRE(v0p, v1p, 8192); });
                SLOTM(sA, sB, pB0, pB1, pA0, pA1, 8192, 8192, true, true, { ABAR(16 * r + t + 1); VPRE(n0p, n1p, 0); });
                ADVANCE();
            }
            {
                BASES();
                SLOTM(sB, sA, pA0, pA1, pB0, pB1, 0, 0, false, true, { VPRE(v0p, v1p, 8192); });
                APPLY_PEND();
                o[0] = MFMA32(vf0, BC8(pB0), o[0]); o[1] = MFMA32(vf1, BC8(pB0), o[1]); vf1 = VRD(8192, 1, 1); vf0 = VRD(8192, 1, 0);
                o[2] = MFMA32(vf2, BC8(pB0), o[2]); o[3] = MFMA32(vf3, BC8(pB0), o[3]); vf3 = VRD(8192, 1, 3); vf2 = VRD(8192, 1, 2);
                o[0] = MFMA32(vf0, BC8(pB1), o[0]); o[1] = MFMA32(vf1, BC8(pB1), o[1]); o[2] = MFMA32(vf2, BC8(pB1), o[2]); o[3] = MFMA32(vf3, BC8(pB1), o[3]);
                if (r == 0) {
                    ETAG();
                    unsigned qa = lds0 + EX + 4096u * qb + lo16; asm volatile("" : "+v"(qa));
#pragma unroll
                    for (int s = 0; s < 8; ++s) qf[s] = ldsrd128((lds_cptr)(uintptr_t)qa + 16384 * (s >> 2) + 1024 * (s & 3));
                }
                const float inv = 1.0f / swap32sum(lsum[0] + lsum[1]);
                int ln = lane; asm volatile("" : "+v"(ln));
                const unsigned ovo = (unsigned)((ln >> 4) & 1) * 4096u + (unsigned)(ln & 15) * 16u + (unsigned)(ln >> 5) * 256u
                                   + (unsigned)((((g * 32 + h0 + r) * 8 + 2 * qb) * 4)) * 1024u;
                unsigned lo_ = lds0 + EX + 4096u * qb + (unsigned)((ln >> 4) & 1) * 16384u + (unsigned)(ln & 15) * 16u + (unsigned)(ln >> 5) * 256u; asm volatile("" : "+v"(lo_));
#pragma unroll
                for (int db = 0; db < 4; ++db)
#pragma unroll
                    for (int sg = 0; sg < 2; ++sg) { const bf16x8 pk_ = pack8s(o[db], sg, inv);
                        if (r == 0) *(LAS bf16x8*)(uintptr_t)(lo_ + 1024u * db + 512u * sg) = pk_; else st16_sc1(BCU(pk_), ovo + 1024u * db + 512u * sg, srdO); }
#pragma unroll
                for (int i = 0; i < 4; ++i) o[i] = f32x16{};
                lsum = f32x2{0.f, 0.f};
            }
        }
        LBAR();
#pragma unroll
        for (int i = 0; i < 2; ++i)
#pragma unroll
            for (int c = 0; c < 9; ++c) oacc[i][c] = f32x4{};
        asm volatile("s_waitcnt vmcnt(0)" ::: "memory");
        if (lane == 0) st4_sc1(tag, WS_FO + (unsigned)((((g * 4 + hq) * 2 + 1) * 4 + qb) * 4 + c4) * 4u, srdF);
    }
    STAMP(4);
    {
        const int mg = w & 3, kq = w >> 2;
        int ln = lane; asm volatile("" : "+v"(ln));
        const unsigned ab0 = lds0 + (unsigned)(16 * ((ln >> 4) >> 1) + 4 * ((ln >> 4) & 1) + ((ln & 15) >> 2)) * 288u + 8u * (ln & 3);
        wait_tag(WS_FO + (unsigned)((((g * 4 + hq) * 2 + 1) * 4 + mg) * 4 + ((c4 + 2 * kq + (lane & 1)) & 3)) * 4u, srdF, tag);
        STAMP(7);
        bf16x8 of[16];
#pragma unroll
        for (int i = 0; i < 8; ++i) {
            const int hd = 8 * hq + 2 * ((c4 + 2 * kq + (i >> 2)) & 3) + 1;
            const unsigned so = (unsigned)((((g * 32 + hd) * 8 + 2 * mg) * 4) + (i & 3)) * 1024u;
            LD16_SC1(of[2 * i], lo16, srdO, RFL(so)); LD16_SC1(of[2 * i + 1], lo16, srdO, RFL(so + 4096u));
        }
        asm volatile("s_waitcnt vmcnt(0)\n\ts_barrier" : "+v"(of[0]), "+v"(of[1]), "+v"(of[2]), "+v"(of[3]), "+v"(of[4]), "+v"(of[5]), "+v"(of[6]), "+v"(of[7]) :: "memory");
        asm volatile("" : "+v"(of[8]), "+v"(of[9]), "+v"(of[10]), "+v"(of[11]), "+v"(of[12]), "+v"(of[13]), "+v"(of[14]), "+v"(of[15]) :: "memory");
        STAMP(2);
#pragma unroll
        for (int jj = 0; jj < 4; ++jj) {
            constexpr unsigned T0[4] = {WS1, WS2, WS0, WS3}, T1[4] = {0u, WCH, 2 * WCH, 3 * WCH};
            unsigned a0a = (kq ? T1[jj] : T0[jj]) + ab0; asm volatile("" : "+v"(a0a));
            const lds_cptr kb_ = (lds_cptr)(uintptr_t)a0a;
            bf16x8 af_[18];
#pragma unroll
            for (int ks = 0; ks < 2; ++ks)
#pragma unroll
                for (int c = 0; c < 9; ++c) af_[9 * ks + c] = cat8(vtr(kb_ + 9216 * ks + 32 * c), vtr(kb_ + 9216 * ks + 2304 + 32 * c));
            SB();
#pragma unroll
            for (int ks = 0; ks < 2; ++ks)
#pragma unroll
                for (int c = 0; c < 9; ++c) {
                    oacc[0][c] = MFMA16(af_[9 * ks + c], of[4 * jj + 2 * ks], oacc[0][c]);
                    oacc[1][c] = MFMA16(af_[9 * ks + c], of[4 * jj + 2 * ks + 1], oacc[1][c]);
                }
            SB();
        }
        LBAR();
        const unsigned xw = (unsigned)w * 9216u + lo16, xr = (unsigned)(w ^ 4) * 9216u + lo16;
#pragma unroll
        for (int c = 0; c < 9; ++c) *(LAS f32x4*)(LW + xw + 1024 * c) = kq ? oacc[0][c] : oacc[1][c];
        LBAR();
        f32x4 fin[9];
#pragma unroll
        for (int c = 0; c < 9; ++c) { const f32x4 t4 = *(const LAS f32x4*)(LW + xr + 1024 * c); fin[c] = (kq ? oacc[1][c] : oacc[0][c]) + t4; }
        const int mb = 2 * mg + kq;
        const unsigned lo8 = (unsigned)lane * 8u;
        STAMP(5);
        if (mg != hq) {
            const unsigned sso = (unsigned)((((g * 4 + c4) * 8 + mb) * 4 + hq) * 9) * 512u + lo8;
#pragma unroll
            for (int c = 0; c < 9; ++c) { const u32x2 v2 = {cvtpk(fin[c][0], fin[c][1]), cvtpk(fin[c][2], fin[c][3])}; st8_sc1(v2, sso + 512u * c, srdS); }
            asm volatile("s_waitcnt vmcnt(0)" ::: "memory");
            if (lane == 0) st4_sc1(tag, WS_FS + (unsigned)(((g * 4 + c4) * 8 + mb) * 4 + hq) * 4u, srdF);
        } else {
            const int ot = (hq + 1 + (lane % 3)) & 3;
            wait_tag(WS_FS + (unsigned)(((g * 4 + c4) * 8 + mb) * 4 + ot) * 4u, srdF, tag);
            if (hq == 0 && c4 == 0 && kq == 0 && lane == 0) st4_sc1(tag, WS_E + 64u * (unsigned)g, srdF);
            u32x2 sv[27];
#pragma unroll
            for (int m = 0; m < 3; ++m)
#pragma unroll
                for (int c = 0; c < 9; ++c) LD8_SC1(sv[9 * m + c], lo8, srdS, RFL((unsigned)((((g * 4 + c4) * 8 + mb) * 4 + ((hq + 1 + m) & 3)) * 9 + c) * 512u));
            asm volatile("s_waitcnt vmcnt(0)" : "+v"(sv[0]), "+v"(sv[1]), "+v"(sv[2]), "+v"(sv[3]), "+v"(sv[4]), "+v"(sv[5]), "+v"(sv[6]), "+v"(sv[7]), "+v"(sv[8]) :: "memory");
            asm volatile("" : "+v"(sv[9]), "+v"(sv[10]), "+v"(sv[11]), "+v"(sv[12]), "+v"(sv[13]), "+v"(sv[14]), "+v"(sv[15]), "+v"(sv[16]), "+v"(sv[17]) :: "memory");
            asm volatile("" : "+v"(sv[18]), "+v"(sv[19]), "+v"(sv[20]), "+v"(sv[21]), "+v"(sv[22]), "+v"(sv[23]), "+v"(sv[24]), "+v"(sv[25]), "+v"(sv[26]) :: "memory");
#pragma unroll
            for (int c = 0; c < 9; ++c) {
                f32x4 a = fin[c];
#pragma unroll
                for (int m = 0; m < 3; ++m) { const u32x2 v = sv[9 * m + c];
                    a[0] += __uint_as_float(v[0] << 16); a[1] += __uint_as_float(v[0] & 0xFFFF0000u); a[2] += __uint_as_float(v[1] << 16); a[3] += __uint_as_float(v[1] & 0xFFFF0000u); }
                const u32x2 ov = {cvtpk(a[0], a[1]), cvtpk(a[2], a[3])};
                *(u32x2*)(out + (size_t)(b * 128 + 16 * mb + (lane & 15)) * 1152 + 576 * p + 144 * c4 + 16 * c + 4 * g4) = ov;
            }
        }
    }
    STAMP(6);
}

extern "C" void kernel_launch(void* const* d_in, const int* in_sizes, int n_in, void* d_out, int out_size, void* d_ws, size_t ws_size, hipStream_t stream) {
    (void)hipFuncSetAttribute((const void*)fused_kernel, hipFuncAttributeMaxDynamicSharedMemorySize, LDS_BYTES);
    hipLaunchKernelGGL(fused_kernel, dim3(256), dim3(512), LDS_BYTES, stream,
                       (const char*)d_in[0], (const char*)d_in[1], (const char*)d_in[2], (const char*)d_in[3], (const char*)d_in[4],
                       (const char*)d_in[5], (const char*)d_in[6], (const char*)d_in[7], (const char*)d_in[8], (const char*)d_in[9],
                       (char*)d_ws, (unsigned short*)d_out);
}
```

```cpp
#include <hip/hip_runtime.h>
#include <hip/hip_bf16.h>
#include <cstdint>

typedef short bf16x8 __attribute__((ext_vector_type(8)));
typedef short s16x4 __attribute__((ext_vector_type(4)));
typedef float f32x16 __attribute__((ext_vector_type(16)));
typedef float f32x4 __attribute__((ext_vector_type(4)));
typedef float f32x2 __attribute__((ext_vector_type(2)));
typedef __bf16 bf16x2_t __attribute__((ext_vector_type(2)));
typedef unsigned u32x4 __attribute__((ext_vector_type(4)));
typedef unsigned u32x2 __attribute__((ext_vector_type(2)));
#define LAS __attribute__((address_space(3)))
#define DI __device__ __forceinline__
typedef LAS const char* lds_cptr;
typedef LAS char* lds_ptr;

constexpr int KVBYTES = 8 * 32 * 1024 * 256;
constexpr unsigned SLOT = 16384;
constexpr int LDS_BYTES = 163840;
constexpr float CSC = 0.08838834764831845f * 1.4426950408889634f;
constexpr float THR = 8.0f;
constexpr unsigned EX = 6 * SLOT, WR = 8 * SLOT;
constexpr unsigned WCH = 18432u, WS0 = 108544u, WS1 = WS0 + WCH, WS2 = WS0 + 2 * WCH, WS3 = WS0 - WCH;
constexpr unsigned WS_E = 0, WS_FO = 4096, WS_FS = 16384;
constexpr size_t WS_O = 65536, WS_O_BYTES = (size_t)2048 * 4096 * 2;
constexpr size_t WS_SL = WS_O + WS_O_BYTES, WS_SL_BYTES = (size_t)16 * 4 * 8 * 4 * 9 * 512;
constexpr unsigned SPIN_CAP = 1u << 20;

#define MFMA32(a, b, c) __builtin_amdgcn_mfma_f32_32x32x16_bf16((a), (b), (c), 0, 0, 0)
#define MFMA16(a, b, c) __builtin_amdgcn_mfma_f32_16x16x32_bf16((a), (b), (c), 0, 0, 0)
#define RFL(x) __builtin_amdgcn_readfirstlane(x)
#define WAITBAR(N) asm volatile("s_waitcnt vmcnt(" #N ") lgkmcnt(0)\n\ts_barrier" ::: "memory")
template <int N> __device__ __forceinline__ void waitbar() { asm volatile("s_waitcnt vmcnt(%0) lgkmcnt(0)\n\ts_barrier" :: "i"(N) : "memory"); }
template <int N> __device__ __forceinline__ void waitv() { asm volatile("s_waitcnt vmcnt(%0)" :: "i"(N) : "memory"); }
#define WB17 WAITBAR(17)
#define PUBT 3
#define WWIN 3
#define XWIN 4
#define LBAR() asm volatile("s_waitcnt lgkmcnt(0)\n\ts_barrier" ::: "memory")
#define SB() __builtin_amdgcn_sched_barrier(0)
#define PIN(x) asm volatile("" : "+v"(x))
#define PIN2(x) asm volatile("" : "+v"(x))

DI unsigned cvtpk(float lo, float hi) { f32x2 v = {lo, hi}; bf16x2_t b = __builtin_convertvector(v, bf16x2_t); return __builtin_bit_cast(unsigned, b); }
DI s16x4 vtr(lds_cptr p) { return __builtin_bit_cast(s16x4, __builtin_amdgcn_ds_read_tr16_b64_v4i16((LAS s16x4*)p)); }
DI bf16x8 cat8(s16x4 a, s16x4 b) { return __builtin_shufflevector(a, b, 0, 1, 2, 3, 4, 5, 6, 7); }
DI bf16x8 ldsrd128(lds_cptr p) { return *(const LAS bf16x8*)p; }
DI float swap32max(float m) { auto rr = __builtin_amdgcn_permlane32_swap(__float_as_uint(m), __float_as_uint(m), false, false); return __builtin_fmaxf(__uint_as_float(rr[0]), __uint_as_float(rr[1])); }
DI float swap32sum(float m) { auto rr = __builtin_amdgcn_permlane32_swap(__float_as_uint(m), __float_as_uint(m), false, false); return __uint_as_float(rr[0]) + __uint_as_float(rr[1]); }
DI bf16x8 pack8s(const f32x16& a, int sg, float sc) {
    u32x4 wv = {cvtpk(a[8 * sg + 0] * sc, a[8 * sg + 1] * sc), cvtpk(a[8 * sg + 2] * sc, a[8 * sg + 3] * sc), cvtpk(a[8 * sg + 4] * sc, a[8 * sg + 5] * sc), cvtpk(a[8 * sg + 6] * sc, a[8 * sg + 7] * sc)};
    return __builtin_bit_cast(bf16x8, wv);
}
#define BC8(x) __builtin_bit_cast(bf16x8, x)
#define BCU(x) __builtin_bit_cast(u32x4, x)

DI void dma16_nt(unsigned ldsdst, unsigned voff, __amdgpu_buffer_rsrc_t srd, unsigned soff) {
    asm volatile("s_mov_b32 m0, %0\n\ts_nop 0\n\tbuffer_load_dwordx4 %1, %2, %3 offen nt lds" :: "s"(ldsdst), "v"(voff), "s"(srd), "s"(soff) : "memory");
}
DI void dma16(unsigned ldsdst, unsigned voff, __amdgpu_buffer_rsrc_t srd, unsigned soff) {
    asm volatile("s_mov_b32 m0, %0\n\ts_nop 0\n\tbuffer_load_dwordx4 %1, %2, %3 offen lds" :: "s"(ldsdst), "v"(voff), "s"(srd), "s"(soff) : "memory");
}
DI void st16_sc1(u32x4 v, unsigned voff, __amdgpu_buffer_rsrc_t srd) { __builtin_amdgcn_raw_buffer_store_b128(v, srd, (int)voff, 0, 16); }
DI void st8_sc1(u32x2 v, unsigned voff, __amdgpu_buffer_rsrc_t srd) { __builtin_amdgcn_raw_buffer_store_b64(v, srd, (int)voff, 0, 16); }
DI void st4_sc1(unsigned v, unsigned voff, __amdgpu_buffer_rsrc_t srd) { __builtin_amdgcn_raw_buffer_store_b32(v, srd, (int)voff, 0, 16); }
#define LD16_SC1(dst, voff, srd, soff) asm volatile("buffer_load_dwordx4 %0, %1, %2, %3 offen sc1" : "=v"(dst) : "v"(voff), "s"(srd), "s"(soff) : "memory")
#define LD8_SC1(dst, voff, srd, soff) asm volatile("buffer_load_dwordx2 %0, %1, %2, %3 offen sc1" : "=v"(dst) : "v"(voff), "s"(srd), "s"(soff) : "memory")
#define LD4_SC1(dst, voff, srd) asm volatile("buffer_load_dword %0, %1, %2, 0 offen sc1" : "=v"(dst) : "v"(voff), "s"(srd) : "memory")
#define LD16(dst, voff, srd, soff) asm volatile("buffer_load_dwordx4 %0, %1, %2, %3 offen" : "=v"(dst) : "v"(voff), "s"(srd), "s"(soff) : "memory")
DI void wait_tag(unsigned voff, __amdgpu_buffer_rsrc_t srd, unsigned tag) {
    for (unsigned sp = 0; sp < SPIN_CAP; ++sp) {
        unsigned f;
        asm volatile("buffer_load_dword %0, %1, %2, 0 offen sc1\n\ts_waitcnt vmcnt(0)" : "=v"(f) : "v"(voff), "s"(srd) : "memory");
        if (__all(f == tag)) break;
        __builtin_amdgcn_s_sleep(1);
    }
}

#define STAMP(i)
#define STAMPT(T)

__global__ __launch_bounds__(512, 2) void fused_kernel(const char* __restrict__ xA, const char* __restrict__ kA, const char* __restrict__ vA, const char* __restrict__ wqA, const char* __restrict__ woA,
                                                      const char* __restrict__ xB, const char* __restrict__ kB, const char* __restrict__ vB, const char* __restrict__ wqB, const char* __restrict__ woB,
                                                      char* __restrict__ ws, unsigned short* __restrict__ out) {
    extern __shared__ __attribute__((aligned(16))) char lds[];
    const int tid = threadIdx.x, lane = tid & 63;
    const int w = RFL(tid >> 6);
    const int r32 = lane & 31, hi = lane >> 5;
    const int wg = blockIdx.x, xcd = wg & 7, idx = wg >> 3;
    const int p = xcd & 1, hq = xcd >> 1, c4 = idx & 3, b = idx >> 2;
    const int g = p * 8 + b;
    const int h0 = 8 * hq + 2 * c4;
    const unsigned lds0 = (unsigned)(uintptr_t)lds;
    const lds_cptr L = (lds_cptr)lds;
    const lds_ptr LW = (lds_ptr)lds;
    STAMP(0);
    const __amdgpu_buffer_rsrc_t srdX = __builtin_amdgcn_make_buffer_rsrc((void*)(p ? xB : xA), (short)0, 8 * 128 * 576 * 2, 0x00020000);
    const __amdgpu_buffer_rsrc_t srdW = __builtin_amdgcn_make_buffer_rsrc((void*)(p ? wqB : wqA), (short)0, 576 * 4096 * 2, 0x00020000);
    const __amdgpu_buffer_rsrc_t srdK = __builtin_amdgcn_make_buffer_rsrc((void*)(p ? kB : kA), (short)0, KVBYTES, 0x00020000);
    const __amdgpu_buffer_rsrc_t srdV = __builtin_amdgcn_make_buffer_rsrc((void*)(p ? vB : vA), (short)0, KVBYTES, 0x00020000);
    const __amdgpu_buffer_rsrc_t srdWo = __builtin_amdgcn_make_buffer_rsrc((void*)(p ? woB : woA), (short)0, 4096 * 576 * 2, 0x00020000);
    const __amdgpu_buffer_rsrc_t srdO = __builtin_amdgcn_make_buffer_rsrc((void*)(ws + WS_O), (short)0, (int)WS_O_BYTES, 0x00020000);
    const __amdgpu_buffer_rsrc_t srdS = __builtin_amdgcn_make_buffer_rsrc((void*)(ws + WS_SL), (short)0, (int)WS_SL_BYTES, 0x00020000);
    const __amdgpu_buffer_rsrc_t srdF = __builtin_amdgcn_make_buffer_rsrc((void*)ws, (short)0, 65536, 0x00020000);

    const bool isB = w >= 4;
    const int bw = w & 3;
    const int r7 = (lane >> 2) & 7;
    const int blk = (lane >> 4) & 1, q4 = (lane & 15) >> 2, p4 = lane & 3, g4 = lane >> 4;
    const unsigned lo16 = (unsigned)lane * 16u;
    unsigned ev;
#define ELOAD() do { const unsigned eo_ = WS_E + 64u * (unsigned)g; asm volatile("buffer_load_dword %0, %1, %2, 0 offen sc1" : "=v"(ev) : "v"(eo_), "s"(srdF) : "memory"); } while (0)
#define ETAG() do { PIN(ev); tag = (unsigned)RFL(ev) * 0x9E3779B1u + 0x7F4A7C15u; } while (0)
    const unsigned chs0 = 16u * (4u * (unsigned)hi + (unsigned)((lane & 3) ^ ((2 * (2 * bw) + (r7 >> 2)) & 3)));
    const unsigned chs1 = 16u * (4u * (unsigned)hi + (unsigned)((lane & 3) ^ ((2 * (2 * bw + 1) + (r7 >> 2)) & 3)));
    const unsigned srcKV0 = (unsigned)(16 * bw + r7) * 256u + chs0, srcKV1 = (unsigned)(16 * bw + 8 + r7) * 256u + chs1;
    const unsigned srcW0 = (unsigned)(16 * bw + r7) * 8192u + chs0, srcW1 = (unsigned)(16 * bw + 8 + r7) * 8192u + chs1;
    auto kv_off = [&](int T) -> unsigned { return (unsigned)((b * 32 + h0) * 1024 + 64 * T) * 256u; };
    auto kslot = [&](int T) -> unsigned { return (unsigned)(T % 3) * SLOT; };
    auto vslot = [&](int T) -> unsigned { return (unsigned)(3 + T % 3) * SLOT; };
    auto dma_tile = [&](__amdgpu_buffer_rsrc_t srd, int T, unsigned slot) { const unsigned so = kv_off(T), d = lds0 + 4096u * (unsigned)bw + slot;
        dma16_nt(RFL(d), srcKV0, srd, RFL(so)); dma16_nt(RFL(d + 1024), srcKV0, srd, RFL(so + 128));
        dma16_nt(RFL(d + 2048), srcKV1, srd, RFL(so)); dma16_nt(RFL(d + 3072), srcKV1, srd, RFL(so + 128)); };
    auto dma_k = [&](int T) { dma_tile(srdK, T, kslot(T)); };
    auto dma_v = [&](int T) { dma_tile(srdV, T, vslot(T)); };
    const int rot = (b + 2 * c4) % 9;
    auto dma_wjob = [&](int head, int n, unsigned dst) {
        const int ps = n / 9, kc0 = n - 9 * ps + rot, kc = kc0 >= 9 ? kc0 - 9 : kc0;
        const unsigned sw = (unsigned)(64 * kc) * 8192u + (unsigned)head * 256u + 128u * (unsigned)ps, d = lds0 + dst + 2048u * (unsigned)bw;
        dma16(RFL(d), srcW0, srdW, RFL(sw)); dma16(RFL(d + 1024), srcW1, srdW, RFL(sw));
    };
    auto dma_wdj = [&](int head, int gj, unsigned dst) { dma_wjob(head, 2 * gj, dst); dma_wjob(head, 2 * gj + 1, dst + 8192u); };
    unsigned tag;

    f32x4 oacc[2][9];
    if (isB) {
        bf16x8 xf[36];
        STAMP(41);
        const unsigned vx = (unsigned)(32 * bw + (lane >> 3)) * 1152u + 16u * (lane & 7);
        const unsigned sx = (unsigned)(b * 128) * 1152u;
#define XLOAD(c) do { const int kk_ = (c) + rot; const unsigned so_ = sx + 128u * (unsigned)(kk_ >= 9 ? kk_ - 9 : kk_); \
            LD16(xf[4 * (c)], vx, srdX, RFL(so_)); LD16(xf[4 * (c) + 1], vx, srdX, RFL(so_ + 9216u)); LD16(xf[4 * (c) + 2], vx, srdX, RFL(so_ + 18432u)); LD16(xf[4 * (c) + 3], vx, srdX, RFL(so_ + 27648u)); } while (0)
        XLOAD(0); XLOAD(1); XLOAD(2); XLOAD(3);
        if (XWIN > 4) XLOAD(4); if (XWIN > 5) XLOAD(5); if (XWIN > 6) XLOAD(6); if (XWIN > 7) XLOAD(7); if (XWIN > 8) XLOAD(8);
        const unsigned xwe = lds0 + EX + 4096u * bw + (unsigned)(lane >> 3) * 128u + 16u * (unsigned)((lane & 7) ^ ((lane >> 4) & 7));
        const unsigned xwo = lds0 + EX + 4096u * bw + (unsigned)(8 + (lane >> 3)) * 128u + 16u * (unsigned)((lane & 7) ^ ((4 + (lane >> 4)) & 7));
        const unsigned xrd = lds0 + EX + 4096u * bw + (unsigned)r32 * 128u + 16u * (unsigned)(hi ^ ((r32 >> 1) & 7));
        const unsigned wb0 = lds0 + 1024u * hi + 64u * (0 + q4) + 16u * ((2 * blk + (p4 & 1)) ^ (2 * hi + 0)) + 8u * (p4 >> 1);
        const unsigned wb1 = lds0 + 1024u * hi + 64u * (4 + q4) + 16u * ((2 * blk + (p4 & 1)) ^ (2 * hi + 1)) + 8u * (p4 >> 1);
        const unsigned qex = EX + 4096u * bw + lo16;
        f32x16 acc[2];
        bf16x8 ar[8];
#define XCONV(c) do { asm volatile("s_waitcnt vmcnt(%4)" : "+v"(xf[4 * (c)]), "+v"(xf[4 * (c) + 1]), "+v"(xf[4 * (c) + 2]), "+v"(xf[4 * (c) + 3]) : "i"(4 * ((((c) - 1 + XWIN < 8) ? (c) - 1 + XWIN : 8) - (c))) : "memory"); \
            *(LAS bf16x8*)(uintptr_t)(xwe) = xf[4 * (c)]; *(LAS bf16x8*)(uintptr_t)(xwo) = xf[4 * (c) + 1]; *(LAS bf16x8*)(uintptr_t)(xwe + 2048) = xf[4 * (c) + 2]; *(LAS bf16x8*)(uintptr_t)(xwo + 2048) = xf[4 * (c) + 3]; \
            asm volatile("" ::: "memory"); \
            _Pragma("unroll") for (int ks = 0; ks < 4; ++ks) xf[4 * (c) + ks] = ldsrd128((lds_cptr)(uintptr_t)(xrd ^ (32u * ks))); \
            asm volatile("" ::: "memory"); \
            if ((c) + XWIN <= 8) XLOAD(((c) + XWIN <= 8) ? (c) + XWIN : 0); } while (0)
#define WFR(wp0, wp1, m) cat8(vtr((wp0) + 8192 * ((m) >> 3) + 2048 * (((m) >> 1) & 3) + 512 * ((m) & 1)), vtr((wp1) + 8192 * ((m) >> 3) + 2048 * (((m) >> 1) & 3) + 512 * ((m) & 1)))
#define WPTR(p0, p1, base) unsigned p0##a = wb0 + (base), p1##a = wb1 + (base); asm volatile("" : "+v"(p0##a), "+v"(p1##a)); const lds_cptr p0 = (lds_cptr)(uintptr_t)p0##a, p1 = (lds_cptr)(uintptr_t)p1##a
#define QMF(gj, m) do { constexpr int n_ = 2 * (gj) + ((m) >> 3), kc_ = n_ % 9, ks_ = ((m) >> 1) & 3, dl_ = (m) & 1; \
            if (kc_ == 0 && ks_ == 0) acc[dl_] = MFMA32(ar[(m) & 7], xf[4 * kc_ + ks_], (f32x16{})); else acc[dl_] = MFMA32(ar[(m) & 7], xf[4 * kc_ + ks_], acc[dl_]); \
            PIN(acc[dl_]); \
            if (kc_ == 8 && ((m) & 7) == 7) { _Pragma("unroll") for (int dl = 0; dl < 2; ++dl) _Pragma("unroll") for (int sg = 0; sg < 2; ++sg) \
                *(LAS bf16x8*)(LW + qex + 16384u * (n_ / 9) + (2 * dl + sg) * 1024) = pack8s(acc[dl], sg, CSC); } } while (0)
#define QSTREAM(gj, base, MID, HASNEXT, nbase) do { \
            { WPTR(c0, c1, base); \
              QMF(gj, 0); ar[6] = WFR(c0, c1, 6); SB(); QMF(gj, 1); ar[7] = WFR(c0, c1, 7); SB(); QMF(gj, 2); ar[0] = WFR(c0, c1, 8); SB(); QMF(gj, 3); ar[1] = WFR(c0, c1, 9); SB(); \
              QMF(gj, 4); ar[2] = WFR(c0, c1, 10); SB(); QMF(gj, 5); ar[3] = WFR(c0, c1, 11); SB(); QMF(gj, 6); ar[4] = WFR(c0, c1, 12); SB(); QMF(gj, 7); ar[5] = WFR(c0, c1, 13); SB(); \
              QMF(gj, 8); ar[6] = WFR(c0, c1, 14); SB(); QMF(gj, 9); ar[7] = WFR(c0, c1, 15); SB(); QMF(gj, 10); SB(); QMF(gj, 11); SB(); } \
            MID; SB(); \
            if (HASNEXT) { WPTR(n0, n1, nbase); \
              QMF(gj, 12); ar[0] = WFR(n0, n1, 0); ar[1] = WFR(n0, n1, 1); SB(); QMF(gj, 13); ar[2] = WFR(n0, n1, 2); ar[3] = WFR(n0, n1, 3); SB(); \
              QMF(gj, 14); SB(); QMF(gj, 15); ar[4] = WFR(n0, n1, 4); ar[5] = WFR(n0, n1, 5); SB(); } \
            else { QMF(gj, 12); SB(); QMF(gj, 13); SB(); QMF(gj, 14); SB(); QMF(gj, 15); SB(); } \
        } while (0)
#define QDJ(T, gj, base) do { STAMPT(T); WPTR(c0, c1, base); \
            dma_k((T) + 2); \
            _Pragma("unroll") for (int m = 0; m < 8; ++m) ar[m] = WFR(c0, c1, m); SB(); \
            QMF(gj, 0); QMF(gj, 1); QMF(gj, 2); QMF(gj, 3); QMF(gj, 4); QMF(gj, 5); QMF(gj, 6); QMF(gj, 7); SB(); \
            dma_v((T) + 1); \
            _Pragma("unroll") for (int m = 8; m < 16; ++m) ar[m & 7] = WFR(c0, c1, m); SB(); \
            QMF(gj, 8); QMF(gj, 9); QMF(gj, 10); QMF(gj, 11); QMF(gj, 12); QMF(gj, 13); QMF(gj, 14); QMF(gj, 15); SB(); } while (0)
        STAMP(1);
        XCONV(0); STAMP(42); XCONV(1);
        LBAR(); STAMP(43);
        { WPTR(c0, c1, 0u); ar[0] = WFR(c0, c1, 0); ar[1] = WFR(c0, c1, 1); ar[2] = WFR(c0, c1, 2); ar[3] = WFR(c0, c1, 3); ar[4] = WFR(c0, c1, 4); ar[5] = WFR(c0, c1, 5); }
        STAMP(44); QSTREAM(0, 0u, LBAR(), true, SLOT);     XCONV(2); XCONV(3); STAMP(45);
        QSTREAM(1, SLOT, LBAR(), true, 3 * SLOT);          XCONV(4); XCONV(5);
        QSTREAM(2, 3 * SLOT, LBAR(), true, 2 * SLOT);      XCONV(6); XCONV(7);
        QSTREAM(3, 2 * SLOT, LBAR(), true, 4 * SLOT);      XCONV(8);
        STAMP(46); QSTREAM(4, 4 * SLOT, LBAR(), true, 5 * SLOT); STAMP(47);
        QSTREAM(5, 5 * SLOT, LBAR(), true, EX + SLOT);
        QSTREAM(6, EX + SLOT, LBAR(), true, WR);
        QSTREAM(7, WR, LBAR(), true, WR + SLOT);
        QSTREAM(8, WR + SLOT, {}, false, 0u);
        STAMP(3);
        LBAR();
#define BKV(T) do { STAMPT(T); if ((T) < 30) dma_k((T) + 2); if ((T) < 31) dma_v((T) + 1); } while (0)
        BKV(0); dma_wdj(h0 + 1, 0, WR); dma_wdj(h0 + 1, 1, WR + SLOT); WAITBAR(16);
        BKV(1); ELOAD(); WAITBAR(9);
        QDJ(2, 0, WR); WAITBAR(8);
        QDJ(3, 1, WR + SLOT); ETAG(); dma_wdj(h0 + 1, 2, WR); WAITBAR(12);
        BKV(4); dma_wdj(h0 + 1, 3, WR + SLOT); WAITBAR(12);
        QDJ(5, 2, WR); WAITBAR(8);
        QDJ(6, 3, WR + SLOT); dma_wdj(h0 + 1, 4, WR); WAITBAR(12);
        BKV(7); dma_wdj(h0 + 1, 5, WR + SLOT); WAITBAR(12);
        QDJ(8, 4, WR); WAITBAR(8);
        QDJ(9, 5, WR + SLOT); dma_wdj(h0 + 1, 6, WR); WAITBAR(12);
        BKV(10); dma_wdj(h0 + 1, 7, WR + SLOT); WAITBAR(12);
        QDJ(11, 6, WR); WAITBAR(8);
        QDJ(12, 7, WR + SLOT); dma_wdj(h0 + 1, 8, WR); WAITBAR(12);
        BKV(13); WAITBAR(8);
        QDJ(14, 8, WR); WAITBAR(8);
        BKV(15); WAITBAR(8);
        {
            int ln = lane; asm volatile("" : "+v"(ln));
            unsigned wov[5];
#pragma unroll
            for (int i = 0; i < 5; ++i) {
                const int pc = (i < 4) ? bw + 4 * i : 16 + (bw & 1);
                const int uu = 64 * pc + ln, pos = uu / 18, ch = uu - 18 * pos;
                const int k = (pos & ~12) | ((pos & 4) << 1) | ((pos & 8) >> 1);
                wov[i] = (unsigned)k * 1152u + 16u * ch;
            }
            const unsigned ab0 = lds0 + (unsigned)(16 * ((ln >> 4) >> 1) + 4 * ((ln >> 4) & 1) + ((ln & 15) >> 2)) * 288u + 8u * (ln & 3);
            auto dma_wo = [&](int r, int jj, unsigned dst) {
                const int hd = 8 * hq + 2 * ((c4 + (jj >> 1)) & 3) + r;
                const unsigned so = (unsigned)(128 * hd + 64 * (jj & 1)) * 1152u + 288u * c4;
#pragma unroll
                for (int i = 0; i < 5; ++i) { const int pc = (i < 4) ? bw + 4 * i : 16 + (bw & 1); dma16(RFL(lds0 + dst + 1024u * pc), wov[i], srdWo, RFL(so)); }
            };
#pragma unroll
            for (int i = 0; i < 2; ++i)
#pragma unroll
                for (int c = 0; c < 9; ++c) oacc[i][c] = f32x4{};
            bf16x8 ofx[4], ofy[4];
            unsigned flagv;
            const unsigned fo1 = WS_FO + (unsigned)((((g * 4 + hq) * 2 + 0) * 4 + bw) * 4 + (ln & 3)) * 4u;
#define O1L(OF, jj) do { const int hd_ = 8 * hq + 2 * ((c4 + ((jj) >> 1)) & 3); const unsigned so_ = (unsigned)((((g * 32 + hd_) * 8 + 2 * bw) * 4) + 2 * ((jj) & 1)) * 1024u; \
                LD16_SC1(OF[0], lo16, srdO, RFL(so_)); LD16_SC1(OF[1], lo16, srdO, RFL(so_ + 4096u)); LD16_SC1(OF[2], lo16, srdO, RFL(so_ + 1024u)); LD16_SC1(OF[3], lo16, srdO, RFL(so_ + 5120u)); } while (0)
#define OPHALF(base, OF, ks) do { \
                unsigned a0a = ab0 + (base) + 9216u * (ks); asm volatile("" : "+v"(a0a)); \
                const lds_cptr kb_ = (lds_cptr)(uintptr_t)a0a; bf16x8 af_[9]; \
                _Pragma("unroll") for (int c = 0; c < 9; ++c) af_[c] = cat8(vtr(kb_ + 32 * c), vtr(kb_ + 2304 + 32 * c)); \
                SB(); \
                _Pragma("unroll") for (int c = 0; c < 9; ++c) { oacc[0][c] = MFMA16(af_[c], OF[2 * (ks)], oacc[0][c]); oacc[1][c] = MFMA16(af_[c], OF[2 * (ks) + 1], oacc[1][c]); PIN(oacc[0][c]); PIN(oacc[1][c]); } \
                SB(); \
            } while (0)
#define WSL(jj) ((jj) % 3 == 0 ? WS2 : (jj) % 3 == 1 ? WS0 : WS1)
#define ISSUE1(jj, OF) do { dma_wo(0, jj, WSL(jj)); O1L(OF, jj); } while (0)
#define BKVC(T, jj, OF) do { STAMPT(T); asm volatile("" : "+v"(OF[0]), "+v"(OF[1]), "+v"(OF[2]), "+v"(OF[3]) :: "memory"); \
                if ((T) < 30) dma_k((T) + 2); OPHALF(WSL(jj), OF, 0); if ((T) < 31) dma_v((T) + 1); OPHALF(WSL(jj), OF, 1); } while (0)
            BKV(16); WAITBAR(8);
            {
                unsigned oa = lds0 + EX + 4096u * bw + lo16; asm volatile("" : "+v"(oa)); const lds_cptr op = (lds_cptr)(uintptr_t)oa;
                ofx[0] = ldsrd128(op); ofx[1] = ldsrd128(op + 16384); ofx[2] = ldsrd128(op + 1024); ofx[3] = ldsrd128(op + 17408);
                ofy[0] = ldsrd128(op + 2048); ofy[1] = ldsrd128(op + 18432); ofy[2] = ldsrd128(op + 3072); ofy[3] = ldsrd128(op + 19456); }
            BKV(17); dma_wo(0, 0, WSL(0)); WAITBAR(13);
            BKV(18); dma_wo(0, 1, WSL(1)); WAITBAR(13);
            BKVC(19, 0, ofx); WAITBAR(8);
            BKVC(20, 1, ofy); WAITBAR(8);
            LD4_SC1(flagv, fo1, srdF);
            BKV(21); WAITBAR(8);
            BKV(22);
            asm volatile("" : "+v"(flagv) :: "memory");
            if (!__all(flagv == tag)) wait_tag(fo1, srdF, tag);
            ISSUE1(2, ofx); WB17;
            BKV(23); ISSUE1(3, ofy); WB17;
            BKVC(24, 2, ofx); ISSUE1(4, ofx); WB17;
            BKVC(25, 3, ofy); ISSUE1(5, ofy); WB17;
            BKVC(26, 4, ofx); ISSUE1(6, ofx); WB17;
            BKVC(27, 5, ofy); ISSUE1(7, ofy); WB17;
            BKVC(28, 6, ofx); dma_wo(1, 0, WS1); WAITBAR(13);
            BKVC(29, 7, ofy); dma_wo(1, 1, WS2); WAITBAR(13);
            BKV(30); dma_wo(1, 2, WS0); WAITBAR(9);
            BKV(31); dma_wo(1, 3, WS3); WAITBAR(5);
            STAMPT(32);
            dma_wo(1, 4, 0u); dma_wo(1, 5, WCH); dma_wo(1, 6, 2u * WCH);
            WAITBAR(15);
            dma_wo(1, 7, 3u * WCH);
        }
    } else {
        {
            constexpr unsigned PS[9] = {0u, SLOT, 3 * SLOT, 2 * SLOT, 4 * SLOT, 5 * SLOT, EX + SLOT, WR, WR + SLOT};
#pragma unroll
            for (int s = 0; s < WWIN; ++s) dma_wdj(h0, s, PS[s]);
            STAMP(41);
#define PSTEP(s) do { waitbar<4 * (((s) - 1 + WWIN < 8 ? (s) - 1 + WWIN : 8) - (s)) + ((s) == 7 ? 4 : (s) == 8 ? 12 : 0)>(); \
                if ((s) + WWIN <= 8) dma_wdj(h0, ((s) + WWIN <= 8) ? (s) + WWIN : 0, PS[((s) + WWIN <= 8) ? (s) + WWIN : 0]); } while (0)
            PSTEP(0); STAMP(42); PSTEP(1); PSTEP(2); PSTEP(3); PSTEP(4); PSTEP(5);
            PSTEP(6); dma_k(0);
            PSTEP(7); dma_k(1); dma_v(0);
            PSTEP(8);
            WAITBAR(8);
            ELOAD();
        }
        const int qb = w;
        bf16x8 qf[8];
        { unsigned qa = lds0 + EX + 4096u * qb + lo16; asm volatile("" : "+v"(qa));
#pragma unroll
          for (int s = 0; s < 8; ++s) qf[s] = ldsrd128((lds_cptr)(uintptr_t)qa + 16384 * (s >> 2) + 1024 * (s & 3)); }
        const unsigned kb0 = 2048u * (r32 >> 3) + 64u * (r32 & 7) + 16u * ((0 + hi) ^ ((r32 >> 2) & 3));
        const unsigned kb1 = 2048u * (r32 >> 3) + 64u * (r32 & 7) + 16u * ((2 + hi) ^ ((r32 >> 2) & 3));
        const unsigned vb0 = 64u * (4 * hi + q4) + 16u * ((2 * blk + (p4 & 1)) ^ (0 + hi)) + 8u * (p4 >> 1);
        const unsigned vb1 = 2048u + 64u * (4 * hi + q4) + 16u * ((2 * blk + (p4 & 1)) ^ (2 + hi)) + 8u * (p4 >> 1);
        f32x16 o[4];
#pragma unroll
        for (int i = 0; i < 4; ++i) o[i] = f32x16{};
        f32x2 lsum = {0.f, 0.f};
        float fpend = 1.f; bool pend = false;
        f32x16 sA = f32x16{}, sB = f32x16{}, negm = f32x16{};
        u32x4 pA0 = {}, pA1 = {}, pB0 = {}, pB1 = {};
        bf16x8 vf0 = {}, vf1 = {}, vf2 = {}, vf3 = {};
        const unsigned kbb0 = lds0 + kb0, kbb1 = lds0 + kb1, vbb0 = lds0 + vb0 + 3 * SLOT, vbb1 = lds0 + vb1 + 3 * SLOT;
        unsigned scur = 0, sprev = 2 * SLOT;
#define KRD(kp, ks) ldsrd128((((ks) & 1) ? k1p : k0p) + (kp) + 512 * ((ks) >> 1))
#define VRDX(a0, a1, vp, sg, db) cat8(vtr((a0) + (vp) + 4096 * (sg) + 512 * (db)), vtr((a1) + (vp) + 4096 * (sg) + 512 * (db)))
#define VRD(vp, sg, db) VRDX(v0p, v1p, vp, sg, db)
#define BASES() asm volatile("" : "+s"(scur), "+s"(sprev)); \
            unsigned k0a = kbb0 + scur, k1a = kbb1 + scur, v0a = vbb0 + sprev, v1a = vbb1 + sprev, n0a = vbb0 + scur, n1a = vbb1 + scur; \
            asm volatile("" : "+v"(k0a), "+v"(k1a), "+v"(v0a), "+v"(v1a), "+v"(n0a), "+v"(n1a)); \
            const lds_cptr k0p = (lds_cptr)(uintptr_t)k0a, k1p = (lds_cptr)(uintptr_t)k1a, v0p = (lds_cptr)(uintptr_t)v0a, v1p = (lds_cptr)(uintptr_t)v1a, n0p = (lds_cptr)(uintptr_t)n0a, n1p = (lds_cptr)(uintptr_t)n1a
#define ADVANCE() do { sprev = scur; scur = (scur == 2 * SLOT) ? 0u : scur + SLOT; } while (0)
#define EX1(S, i) do { S[i] = __builtin_amdgcn_exp2f(S[i]); } while (0)
#define MX3(a, b, c) __builtin_fmaxf(__builtin_fmaxf(a, b), c)
#define AMAX3(d, a, b, c) asm volatile("v_max3_f32 %0, %1, %2, %3" : "=v"(d) : "v"(a), "v"(b), "v"(c))
#define AMAX2(d, a, b) asm volatile("v_max_f32 %0, %1, %2" : "=v"(d) : "v"(a), "v"(b))
#define PAIR(S, i) (f32x2{S[2 * (i)], S[2 * (i) + 1]})
#define RMAX1(S) ma_ = MX3(S[0], S[1], S[2]); mb_ = MX3(S[3], S[4], S[5]); mc_ = MX3(S[6], S[7], S[8]); md_ = MX3(S[9], S[10], S[11]); me_ = MX3(S[12], S[13], S[14]); \
            ma_ = MX3(ma_, mb_, mc_); md_ = MX3(md_, me_, S[15]); ma_ = MX3(ma_, md_, 0.f); PIN(ma_)
#define RMAX2() do { auto rr_ = __builtin_amdgcn_permlane32_swap(__float_as_uint(ma_), __float_as_uint(ma_), false, false); \
            rm_ = MX3(__uint_as_float(rr_[0]), __uint_as_float(rr_[1]), 0.f); PIN(rm_); } while (0)
#define APPLY_PEND() do { if (__builtin_expect(pend, 0)) { _Pragma("unroll") for (int d = 0; d < 4; ++d) _Pragma("unroll") for (int i = 0; i < 16; ++i) o[d][i] *= fpend; pend = false; } } while (0)
#define DECIDE(SN, PP1, DOPV) do { \
            if (__builtin_expect(__any(rm_ > THR), 0)) { const float dl_ = rm_; \
                _Pragma("unroll") for (int i = 0; i < 16; ++i) { SN[i] -= dl_; negm[i] -= dl_; } \
                fpend = __builtin_amdgcn_exp2f(-dl_); lsum *= fpend; pend = true; } \
        } while (0)
#define SLOTM(SC, SN, PP0, PP1, PC0, PC1, kp, vp, DOQK, DOPV, MID) do { \
            float rm_, ma_, mb_, mc_, md_, me_; f32x2 s0_, s1_; \
            bf16x8 k0_, k1_, k2_, k3_; \
            if (DOQK) { k1_ = KRD(kp, 1); k0_ = KRD(kp, 0); k3_ = KRD(kp, 3); k2_ = KRD(kp, 2); } \
            SB(); \
            APPLY_PEND(); \
              if (DOPV) o[0] = MFMA32(vf0, BC8(PP0), o[0]); RMAX1(SC); SB(); \
              if (DOPV) { o[1] = MFMA32(vf1, BC8(PP0), o[1]); vf1 = VRD(vp, 1, 1); vf0 = VRD(vp, 1, 0); } RMAX2(); SB(); \
              if (DOPV) o[2] = MFMA32(vf2, BC8(PP0), o[2]); SB(); \
              if (DOPV) { o[3] = MFMA32(vf3, BC8(PP0), o[3]); vf3 = VRD(vp, 1, 3); vf2 = VRD(vp, 1, 2); } SB(); \
            DECIDE(SC, PP1, DOPV); SB(); \
              if (DOQK) SN = MFMA32(k0_, qf[0], negm); EX1(SC, 0); EX1(SC, 1); PIN(SC); SB(); \
              if (DOPV) o[0] = MFMA32(vf0, BC8(PP1), o[0]); EX1(SC, 2); EX1(SC, 3); PC0[0] = cvtpk(SC[0], SC[1]); PIN(SC); PIN(PC0); SB(); \
              if (DOQK) { SN = MFMA32(k1_, qf[1], SN); k1_ = KRD(kp, 5); k0_ = KRD(kp, 4); } s0_ = PAIR(SC, 0) + PAIR(SC, 1); PIN2(s0_); EX1(SC, 4); EX1(SC, 5); PIN(SC); SB(); \
              if (DOPV) o[1] = MFMA32(vf1, BC8(PP1), o[1]); PC0[1] = cvtpk(SC[2], SC[3]); EX1(SC, 6); PC0[2] = cvtpk(SC[4], SC[5]); PIN(SC); PIN(PC0); SB(); \
              if (DOQK) SN = MFMA32(k2_, qf[2], SN); s0_ += PAIR(SC, 2); PIN2(s0_); EX1(SC, 7); EX1(SC, 8); PIN(SC); SB(); \
              if (DOPV) o[2] = MFMA32(vf2, BC8(PP1), o[2]); EX1(SC, 9); PC0[3] = cvtpk(SC[6], SC[7]); PIN(SC); PIN(PC0); SB(); \
              if (DOQK) { SN = MFMA32(k3_, qf[3], SN); k3_ = KRD(kp, 7); k2_ = KRD(kp, 6); } s1_ = PAIR(SC, 3) + PAIR(SC, 4); PIN2(s1_); EX1(SC, 10); EX1(SC, 11); PIN(SC); SB(); \
              if (DOPV) o[3] = MFMA32(vf3, BC8(PP1), o[3]); PC1[0] = cvtpk(SC[8], SC[9]); EX1(SC, 12); PC1[1] = cvtpk(SC[10], SC[11]); PIN(SC); PIN(PC1); SB(); \
              if (DOQK) SN = MFMA32(k0_, qf[4], SN); s1_ += PAIR(SC, 5); PIN2(s1_); EX1(SC, 13); EX1(SC, 14); PIN(SC); SB(); \
              if (DOQK) SN = MFMA32(k1_, qf[5], SN); EX1(SC, 15); PC1[2] = cvtpk(SC[12], SC[13]); PIN(SC); PIN(PC1); SB(); \
            MID; SB(); \
              if (DOQK) SN = MFMA32(k2_, qf[6], SN); s0_ += PAIR(SC, 6); s1_ += PAIR(SC, 7); PIN2(s0_); PIN2(s1_); PC1[3] = cvtpk(SC[14], SC[15]); PIN(PC1); SB(); \
              if (DOQK) SN = MFMA32(k3_, qf[7], SN); lsum += s0_; lsum += s1_; PIN2(lsum); SB(); \
        } while (0)
#define VPRE(a0, a1, vp) do { vf1 = VRDX(a0, a1, vp, 0, 1); vf0 = VRDX(a0, a1, vp, 0, 0); vf3 = VRDX(a0, a1, vp, 0, 3); vf2 = VRDX(a0, a1, vp, 0, 2); } while (0)
#define WT0()
#define WT1(Tn)
#define ABAR(Tn) do { WT0(); LBAR(); WT1(Tn); STAMPT(Tn); } while (0)
        STAMP(8);
#pragma unroll 1
        for (int r = 0; r < 2; ++r) {
            {
                BASES();
#pragma unroll
                for (int ks = 0; ks < 8; ++ks) sA = MFMA32(KRD(0, ks), qf[ks], ks ? sA : (f32x16{}));
                float rm; { float a_ = MX3(sA[0], sA[1], sA[2]), b_ = MX3(sA[3], sA[4], sA[5]); a_ = MX3(a_, sA[6], sA[7]); b_ = MX3(b_, sA[8], sA[9]); a_ = MX3(a_, sA[10], sA[11]); b_ = MX3(b_, sA[12], sA[13]);
                    a_ = MX3(a_, sA[14], sA[15]); rm = swap32max(__builtin_fmaxf(a_, b_)); }
#pragma unroll
                for (int i = 0; i < 16; ++i) { sA[i] -= rm; negm[i] = -rm; }
                pend = false;
                SLOTM(sA, sB, pB0, pB1, pA0, pA1, 8192, 0, true, false, { if (r == 0) asm volatile("s_waitcnt vmcnt(0)" ::: "memory");
                    ABAR(16 * r + 1); VPRE(n0p, n1p, 0); });
                ADVANCE();
            }
#pragma unroll 1
            for (int t = 1; t < 16; ++t) {
                if (r == 1 && t == PUBT) {
                    asm volatile("s_waitcnt vmcnt(0)" ::: "memory");
                    if (lane == 0) st4_sc1(tag, WS_FO + (unsigned)((((g * 4 + hq) * 2 + 0) * 4 + qb) * 4 + c4) * 4u, srdF);
                }
                BASES();
                SLOTM(sB, sA, pA0, pA1, pB0, pB1, 0, 0, true, true, { VPRE(v0p, v1p, 8192); });
                SLOTM(sA, sB, pB0, pB1, pA0, pA1, 8192, 8192, true, true, { ABAR(16 * r + t + 1); VPRE(n0p, n1p, 0); });
                ADVANCE();
            }
            {
                BASES();
                SLOTM(sB, sA, pA0, pA1, pB0, pB1, 0, 0, false, true, { VPRE(v0p, v1p, 8192); });
                APPLY_PEND();
                o[0] = MFMA32(vf0, BC8(pB0), o[0]); o[1] = MFMA32(vf1, BC8(pB0), o[1]); vf1 = VRD(8192, 1, 1); vf0 = VRD(8192, 1, 0);
                o[2] = MFMA32(vf2, BC8(pB0), o[2]); o[3] = MFMA32(vf3, BC8(pB0), o[3]); vf3 = VRD(8192, 1, 3); vf2 = VRD(8192, 1, 2);
                o[0] = MFMA32(vf0, BC8(pB1), o[0]); o[1] = MFMA32(vf1, BC8(pB1), o[1]); o[2] = MFMA32(vf2, BC8(pB1), o[2]); o[3] = MFMA32(vf3, BC8(pB1), o[3]);
                if (r == 0) {
                    ETAG();
                    unsigned qa = lds0 + EX + 4096u * qb + lo16; asm volatile("" : "+v"(qa));
#pragma unroll
                    for (int s = 0; s < 8; ++s) qf[s] = ldsrd128((lds_cptr)(uintptr_t)qa + 16384 * (s >> 2) + 1024 * (s & 3));
                }
                const float inv = 1.0f / swap32sum(lsum[0] + lsum[1]);
                int ln = lane; asm volatile("" : "+v"(ln));
                const unsigned ovo = (unsigned)((ln >> 4) & 1) * 4096u + (unsigned)(ln & 15) * 16u + (unsigned)(ln >> 5) * 256u
                                   + (unsigned)((((g * 32 + h0 + r) * 8 + 2 * qb) * 4)) * 1024u;
                unsigned lo_ = lds0 + EX + 4096u * qb + (unsigned)((ln >> 4) & 1) * 16384u + (unsigned)(ln & 15) * 16u + (unsigned)(ln >> 5) * 256u; asm volatile("" : "+v"(lo_));
#pragma unroll
                for (int db = 0; db < 4; ++db)
#pragma unroll
                    for (int sg = 0; sg < 2; ++sg) { const bf16x8 pk_ = pack8s(o[db], sg, inv);
                        if (r == 0) *(LAS bf16x8*)(uintptr_t)(lo_ + 1024u * db + 512u * sg) = pk_;
                        st16_sc1(BCU(pk_), ovo + 1024u * db + 512u * sg, srdO); }
#pragma unroll
                for (int i = 0; i < 4; ++i) o[i] = f32x16{};
                lsum = f32x2{0.f, 0.f};
            }
        }
        LBAR();
#pragma unroll
        for (int i = 0; i < 2; ++i)
#pragma unroll
            for (int c = 0; c < 9; ++c) oacc[i][c] = f32x4{};
        asm volatile("s_waitcnt vmcnt(0)" ::: "memory");
        if (lane == 0) st4_sc1(tag, WS_FO + (unsigned)((((g * 4 + hq) * 2 + 1) * 4 + qb) * 4 + c4) * 4u, srdF);
    }
    STAMP(4);
    {
        const int mg = w & 3, kq = w >> 2;
        int ln = lane; asm volatile("" : "+v"(ln));
        const unsigned ab0 = lds0 + (unsigned)(16 * ((ln >> 4) >> 1) + 4 * ((ln >> 4) & 1) + ((ln & 15) >> 2)) * 288u + 8u * (ln & 3);
        wait_tag(WS_FO + (unsigned)((((g * 4 + hq) * 2 + 1) * 4 + mg) * 4 + ((c4 + 2 * kq + (lane & 1)) & 3)) * 4u, srdF, tag);
        STAMP(7);
        bf16x8 of[16];
#pragma unroll
        for (int i = 0; i < 8; ++i) {
            const int hd = 8 * hq + 2 * ((c4 + 2 * kq + (i >> 2)) & 3) + 1;
            const unsigned so = (unsigned)((((g * 32 + hd) * 8 + 2 * mg) * 4) + (i & 3)) * 1024u;
            LD16_SC1(of[2 * i], lo16, srdO, RFL(so)); LD16_SC1(of[2 * i + 1], lo16, srdO, RFL(so + 4096u));
        }
        asm volatile("s_waitcnt vmcnt(0)\n\ts_barrier" : "+v"(of[0]), "+v"(of[1]), "+v"(of[2]), "+v"(of[3]), "+v"(of[4]), "+v"(of[5]), "+v"(of[6]), "+v"(of[7]) :: "memory");
        asm volatile("" : "+v"(of[8]), "+v"(of[9]), "+v"(of[10]), "+v"(of[11]), "+v"(of[12]), "+v"(of[13]), "+v"(of[14]), "+v"(of[15]) :: "memory");
        STAMP(2);
#pragma unroll
        for (int jj = 0; jj < 4; ++jj) {
            constexpr unsigned T0[4] = {WS1, WS2, WS0, WS3}, T1[4] = {0u, WCH, 2 * WCH, 3 * WCH};
            unsigned a0a = (kq ? T1[jj] : T0[jj]) + ab0; asm volatile("" : "+v"(a0a));
            const lds_cptr kb_ = (lds_cptr)(uintptr_t)a0a;
            bf16x8 af_[18];
#pragma unroll
            for (int ks = 0; ks < 2; ++ks)
#pragma unroll
                for (int c = 0; c < 9; ++c) af_[9 * ks + c] = cat8(vtr(kb_ + 9216 * ks + 32 * c), vtr(kb_ + 9216 * ks + 2304 + 32 * c));
            SB();
#pragma unroll
            for (int ks = 0; ks < 2; ++ks)
#pragma unroll
                for (int c = 0; c < 9; ++c) {
                    oacc[0][c] = MFMA16(af_[9 * ks + c], of[4 * jj + 2 * ks], oacc[0][c]);
                    oacc[1][c] = MFMA16(af_[9 * ks + c], of[4 * jj + 2 * ks + 1], oacc[1][c]);
                }
            SB();
        }
        LBAR();
        const unsigned xw = (unsigned)w * 9216u + lo16, xr = (unsigned)(w ^ 4) * 9216u + lo16;
#pragma unroll
        for (int c = 0; c < 9; ++c) *(LAS f32x4*)(LW + xw + 1024 * c) = kq ? oacc[0][c] : oacc[1][c];
        LBAR();
        f32x4 fin[9];
#pragma unroll
        for (int c = 0; c < 9; ++c) { const f32x4 t4 = *(const LAS f32x4*)(LW + xr + 1024 * c); fin[c] = (kq ? oacc[1][c] : oacc[0][c]) + t4; }
        const int mb = 2 * mg + kq;
        const unsigned lo8 = (unsigned)lane * 8u;
        STAMP(5);
        if (mg != hq) {
            const unsigned sso = (unsigned)((((g * 4 + c4) * 8 + mb) * 4 + hq) * 9) * 512u + lo8;
#pragma unroll
            for (int c = 0; c < 9; ++c) { const u32x2 v2 = {cvtpk(fin[c][0], fin[c][1]), cvtpk(fin[c][2], fin[c][3])}; st8_sc1(v2, sso + 512u * c, srdS); }
            asm volatile("s_waitcnt vmcnt(0)" ::: "memory");
            if (lane == 0) st4_sc1(tag, WS_FS + (unsigned)(((g * 4 + c4) * 8 + mb) * 4 + hq) * 4u, srdF);
        } else {
            const int ot = (hq + 1 + (lane % 3)) & 3;
            wait_tag(WS_FS + (unsigned)(((g * 4 + c4) * 8 + mb) * 4 + ot) * 4u, srdF, tag);
            if (hq == 0 && c4 == 0 && kq == 0 && lane == 0) st4_sc1(tag, WS_E + 64u * (unsigned)g, srdF);
            u32x2 sv[27];
#pragma unroll
            for (int m = 0; m < 3; ++m)
#pragma unroll
                for (int c = 0; c < 9; ++c) LD8_SC1(sv[9 * m + c], lo8, srdS, RFL((unsigned)((((g * 4 + c4) * 8 + mb) * 4 + ((hq + 1 + m) & 3)) * 9 + c) * 512u));
            asm volatile("s_waitcnt vmcnt(0)" : "+v"(sv[0]), "+v"(sv[1]), "+v"(sv[2]), "+v"(sv[3]), "+v"(sv[4]), "+v"(sv[5]), "+v"(sv[6]), "+v"(sv[7]), "+v"(sv[8]) :: "memory");
            asm volatile("" : "+v"(sv[9]), "+v"(sv[10]), "+v"(sv[11]), "+v"(sv[12]), "+v"(sv[13]), "+v"(sv[14]), "+v"(sv[15]), "+v"(sv[16]), "+v"(sv[17]) :: "memory");
            asm volatile("" : "+v"(sv[18]), "+v"(sv[19]), "+v"(sv[20]), "+v"(sv[21]), "+v"(sv[22]), "+v"(sv[23]), "+v"(sv[24]), "+v"(sv[25]), "+v"(sv[26]) :: "memory");
#pragma unroll
            for (int c = 0; c < 9; ++c) {
                f32x4 a = fin[c];
#pragma unroll
                for (int m = 0; m < 3; ++m) { const u32x2 v = sv[9 * m + c];
                    a[0] += __uint_as_float(v[0] << 16); a[1] += __uint_as_float(v[0] & 0xFFFF0000u); a[2] += __uint_as_float(v[1] << 16); a[3] += __uint_as_float(v[1] & 0xFFFF0000u); }
                const u32x2 ov = {cvtpk(a[0], a[1]), cvtpk(a[2], a[3])};
                *(u32x2*)(out + (size_t)(b * 128 + 16 * mb + (lane & 15)) * 1152 + 576 * p + 144 * c4 + 16 * c + 4 * g4) = ov;
            }
        }
    }
    STAMP(6);
}

extern "C" void kernel_launch(void* const* d_in, const int* in_sizes, int n_in, void* d_out, int out_size, void* d_ws, size_t ws_size, hipStream_t stream) {
    (void)hipFuncSetAttribute((const void*)fused_kernel, hipFuncAttributeMaxDynamicSharedMemorySize, LDS_BYTES);
    hipLaunchKernelGGL(fused_kernel, dim3(256), dim3(512), LDS_BYTES, stream,
                       (const char*)d_in[0], (const char*)d_in[1], (const char*)d_in[2], (const char*)d_in[3], (const char*)d_in[4],
                       (const char*)d_in[5], (const char*)d_in[6], (const char*)d_in[7], (const char*)d_in[8], (const char*)d_in[9],
                       (char*)d_ws, (unsigned short*)d_out);
}
```

```cpp
#include <hip/hip_runtime.h>
#include <hip/hip_bf16.h>
#include <cstdint>

typedef short bf16x8 __attribute__((ext_vector_type(8)));
typedef short s16x4 __attribute__((ext_vector_type(4)));
typedef float f32x16 __attribute__((ext_vector_type(16)));
typedef float f32x4 __attribute__((ext_vector_type(4)));
typedef float f32x2 __attribute__((ext_vector_type(2)));
typedef __bf16 bf16x2_t __attribute__((ext_vector_type(2)));
typedef unsigned u32x4 __attribute__((ext_vector_type(4)));
typedef unsigned u32x2 __attribute__((ext_vector_type(2)));
#define LAS __attribute__((address_space(3)))
#define DI __device__ __forceinline__
typedef LAS const char* lds_cptr;
typedef LAS char* lds_ptr;

constexpr int KVBYTES = 8 * 32 * 1024 * 256;
constexpr unsigned SLOT = 16384;
constexpr int LDS_BYTES = 163840;
constexpr float CSC = 0.08838834764831845f * 1.4426950408889634f;
constexpr float THR = 8.0f;
constexpr unsigned EX = 6 * SLOT, WR = 8 * SLOT;
constexpr unsigned WCH = 18432u, WS0 = 108544u, WS1 = WS0 + WCH, WS2 = WS0 + 2 * WCH, WS3 = WS0 - WCH;
constexpr unsigned WS_E = 0, WS_FO = 4096, WS_FS = 16384;
constexpr size_t WS_O = 65536, WS_O_BYTES = (size_t)2048 * 4096 * 2;
constexpr size_t WS_SL = WS_O + WS_O_BYTES, WS_SL_BYTES = (size_t)16 * 4 * 8 * 4 * 9 * 512;
constexpr unsigned SPIN_CAP = 1u << 20;

#define MFMA32(a, b, c) __builtin_amdgcn_mfma_f32_32x32x16_bf16((a), (b), (c), 0, 0, 0)
#define MFMA16(a, b, c) __builtin_amdgcn_mfma_f32_16x16x32_bf16((a), (b), (c), 0, 0, 0)
#define RFL(x) __builtin_amdgcn_readfirstlane(x)
#define WAITBAR(N) asm volatile("s_waitcnt vmcnt(" #N ") lgkmcnt(0)\n\ts_barrier" ::: "memory")
template <int N> __device__ __forceinline__ void waitbar() { asm volatile("s_waitcnt vmcnt(%0) lgkmcnt(0)\n\ts_barrier" :: "i"(N) : "memory"); }
template <int N> __device__ __forceinline__ void waitv() { asm volatile("s_waitcnt vmcnt(%0)" :: "i"(N) : "memory"); }
#define WB17 WAITBAR(17)
#define WWIN 3
#define XWIN 4
#define LBAR() asm volatile("s_waitcnt lgkmcnt(0)\n\ts_barrier" ::: "memory")
#define SB() __builtin_amdgcn_sched_barrier(0)
#define PIN(x) asm volatile("" : "+v"(x))
#define PIN2(x) asm volatile("" : "+v"(x))

DI unsigned cvtpk(float lo, float hi) { f32x2 v = {lo, hi}; bf16x2_t b = __builtin_convertvector(v, bf16x2_t); return __builtin_bit_cast(unsigned, b); }
DI s16x4 vtr(lds_cptr p) { return __builtin_bit_cast(s16x4, __builtin_amdgcn_ds_read_tr16_b64_v4i16((LAS s16x4*)p)); }
DI bf16x8 cat8(s16x4 a, s16x4 b) { return __builtin_shufflevector(a, b, 0, 1, 2, 3, 4, 5, 6, 7); }
DI bf16x8 ldsrd128(lds_cptr p) { return *(const LAS bf16x8*)p; }
DI float swap32max(float m) { auto rr = __builtin_amdgcn_permlane32_swap(__float_as_uint(m), __float_as_uint(m), false, false); return __builtin_fmaxf(__uint_as_float(rr[0]), __uint_as_float(rr[1])); }
DI float swap32sum(float m) { auto rr = __builtin_amdgcn_permlane32_swap(__float_as_uint(m), __float_as_uint(m), false, false); return __uint_as_float(rr[0]) + __uint_as_float(rr[1]); }
DI bf16x8 pack8s(const f32x16& a, int sg, float sc) {
    u32x4 wv = {cvtpk(a[8 * sg + 0] * sc, a[8 * sg + 1] * sc), cvtpk(a[8 * sg + 2] * sc, a[8 * sg + 3] * sc), cvtpk(a[8 * sg + 4] * sc, a[8 * sg + 5] * sc), cvtpk(a[8 * sg + 6] * sc, a[8 * sg + 7] * sc)};
    return __builtin_bit_cast(bf16x8, wv);
}
#define BC8(x) __builtin_bit_cast(bf16x8, x)
#define BCU(x) __builtin_bit_cast(u32x4, x)

DI void dma16_nt(unsigned ldsdst, unsigned voff, __amdgpu_buffer_rsrc_t srd, unsigned soff) {
    asm volatile("s_mov_b32 m0, %0\n\ts_nop 0\n\tbuffer_load_dwordx4 %1, %2, %3 offen nt lds" :: "s"(ldsdst), "v"(voff), "s"(srd), "s"(soff) : "memory");
}
DI void dma16(unsigned ldsdst, unsigned voff, __amdgpu_buffer_rsrc_t srd, unsigned soff) {
    asm volatile("s_mov_b32 m0, %0\n\ts_nop 0\n\tbuffer_load_dwordx4 %1, %2, %3 offen lds" :: "s"(ldsdst), "v"(voff), "s"(srd), "s"(soff) : "memory");
}
DI void st16_sc1(u32x4 v, unsigned voff, __amdgpu_buffer_rsrc_t srd) { __builtin_amdgcn_raw_buffer_store_b128(v, srd, (int)voff, 0, 16); }
DI void st8_sc1(u32x2 v, unsigned voff, __amdgpu_buffer_rsrc_t srd) { __builtin_amdgcn_raw_buffer_store_b64(v, srd, (int)voff, 0, 16); }
DI void st4_sc1(unsigned v, unsigned voff, __amdgpu_buffer_rsrc_t srd) { __builtin_amdgcn_raw_buffer_store_b32(v, srd, (int)voff, 0, 16); }
#define LD16_SC1(dst, voff, srd, soff) asm volatile("buffer_load_dwordx4 %0, %1, %2, %3 offen sc1" : "=v"(dst) : "v"(voff), "s"(srd), "s"(soff) : "memory")
#define LD8_SC1(dst, voff, srd, soff) asm volatile("buffer_load_dwordx2 %0, %1, %2, %3 offen sc1" : "=v"(dst) : "v"(voff), "s"(srd), "s"(soff) : "memory")
#define LD4_SC1(dst, voff, srd) asm volatile("buffer_load_dword %0, %1, %2, 0 offen sc1" : "=v"(dst) : "v"(voff), "s"(srd) : "memory")
#define LD16(dst, voff, srd, soff) asm volatile("buffer_load_dwordx4 %0, %1, %2, %3 offen" : "=v"(dst) : "v"(voff), "s"(srd), "s"(soff) : "memory")
DI void wait_tag(unsigned voff, __amdgpu_buffer_rsrc_t srd, unsigned tag) {
    for (unsigned sp = 0; sp < SPIN_CAP; ++sp) {
        unsigned f;
        asm volatile("buffer_load_dword %0, %1, %2, 0 offen sc1\n\ts_waitcnt vmcnt(0)" : "=v"(f) : "v"(voff), "s"(srd) : "memory");
        if (__all(f == tag)) break;
        __builtin_amdgcn_s_sleep(1);
    }
}

__global__ __launch_bounds__(512, 2) void fused_kernel(const char* __restrict__ xA, const char* __restrict__ kA, const char* __restrict__ vA, const char* __restrict__ wqA, const char* __restrict__ woA,
                                                      const char* __restrict__ xB, const char* __restrict__ kB, const char* __restrict__ vB, const char* __restrict__ wqB, const char* __restrict__ woB,
                                                      char* __restrict__ ws, unsigned short* __restrict__ out) {
    extern __shared__ __attribute__((aligned(16))) char lds[];
    const int tid = threadIdx.x, lane = tid & 63;
    const int w = RFL(tid >> 6);
    const int r32 = lane & 31, hi = lane >> 5;
    const int wg = blockIdx.x, xcd = wg & 7, idx = wg >> 3;
    const int p = xcd & 1, hq = xcd >> 1, c4 = idx & 3, b = idx >> 2;
    const int g = p * 8 + b;
    const int h0 = 8 * hq + 2 * c4;
    const unsigned lds0 = (unsigned)(uintptr_t)lds;
    const lds_cptr L = (lds_cptr)lds;
    const lds_ptr LW = (lds_ptr)lds;
        const __amdgpu_buffer_rsrc_t srdX = __builtin_amdgcn_make_buffer_rsrc((void*)(p ? xB : xA), (short)0, 8 * 128 * 576 * 2, 0x00020000);
    const __amdgpu_buffer_rsrc_t srdW = __builtin_amdgcn_make_buffer_rsrc((void*)(p ? wqB : wqA), (short)0, 576 * 4096 * 2, 0x00020000);
    const __amdgpu_buffer_rsrc_t srdK = __builtin_amdgcn_make_buffer_rsrc((void*)(p ? kB : kA), (short)0, KVBYTES, 0x00020000);
    const __amdgpu_buffer_rsrc_t srdV = __builtin_amdgcn_make_buffer_rsrc((void*)(p ? vB : vA), (short)0, KVBYTES, 0x00020000);
    const __amdgpu_buffer_rsrc_t srdWo = __builtin_amdgcn_make_buffer_rsrc((void*)(p ? woB : woA), (short)0, 4096 * 576 * 2, 0x00020000);
    const __amdgpu_buffer_rsrc_t srdO = __builtin_amdgcn_make_buffer_rsrc((void*)(ws + WS_O), (short)0, (int)WS_O_BYTES, 0x00020000);
    const __amdgpu_buffer_rsrc_t srdS = __builtin_amdgcn_make_buffer_rsrc((void*)(ws + WS_SL), (short)0, (int)WS_SL_BYTES, 0x00020000);
    const __amdgpu_buffer_rsrc_t srdF = __builtin_amdgcn_make_buffer_rsrc((void*)ws, (short)0, 65536, 0x00020000);

    const bool isB = w >= 4;
    const int bw = w & 3;
    const int r7 = (lane >> 2) & 7;
    const int blk = (lane >> 4) & 1, q4 = (lane & 15) >> 2, p4 = lane & 3, g4 = lane >> 4;
    const unsigned lo16 = (unsigned)lane * 16u;
    unsigned ev;
#define ELOAD() do { const unsigned eo_ = WS_E + 64u * (unsigned)g; asm volatile("buffer_load_dword %0, %1, %2, 0 offen sc1" : "=v"(ev) : "v"(eo_), "s"(srdF) : "memory"); } while (0)
#define ETAG() do { PIN(ev); tag = (unsigned)RFL(ev) * 0x9E3779B1u + 0x7F4A7C15u; } while (0)
    const unsigned chs0 = 16u * (4u * (unsigned)hi + (unsigned)((lane & 3) ^ ((2 * (2 * bw) + (r7 >> 2)) & 3)));
    const unsigned chs1 = 16u * (4u * (unsigned)hi + (unsigned)((lane & 3) ^ ((2 * (2 * bw + 1) + (r7 >> 2)) & 3)));
    const unsigned srcKV0 = (unsigned)(16 * bw + r7) * 256u + chs0, srcKV1 = (unsigned)(16 * bw + 8 + r7) * 256u + chs1;
    const unsigned srcW0 = (unsigned)(16 * bw + r7) * 8192u + chs0, srcW1 = (unsigned)(16 * bw + 8 + r7) * 8192u + chs1;
    auto kv_off = [&](int T) -> unsigned { return (unsigned)((b * 32 + h0) * 1024 + 64 * T) * 256u; };
    auto kslot = [&](int T) -> unsigned { return (unsigned)(T % 3) * SLOT; };
    auto vslot = [&](int T) -> unsigned { return (unsigned)(3 + T % 3) * SLOT; };
    auto dma_tile = [&](__amdgpu_buffer_rsrc_t srd, int T, unsigned slot) { const unsigned so = kv_off(T), d = lds0 + 4096u * (unsigned)bw + slot;
        dma16_nt(RFL(d), srcKV0, srd, RFL(so)); dma16_nt(RFL(d + 1024), srcKV0, srd, RFL(so + 128));
        dma16_nt(RFL(d + 2048), srcKV1, srd, RFL(so)); dma16_nt(RFL(d + 3072), srcKV1, srd, RFL(so + 128)); };
    auto dma_k = [&](int T) { dma_tile(srdK, T, kslot(T)); };
    auto dma_v = [&](int T) { dma_tile(srdV, T, vslot(T)); };
    const int rot = (b + 2 * c4) % 9;
    auto dma_wjob = [&](int head, int n, unsigned dst) {
        const int ps = n / 9, kc0 = n - 9 * ps + rot, kc = kc0 >= 9 ? kc0 - 9 : kc0;
        const unsigned sw = (unsigned)(64 * kc) * 8192u + (unsigned)head * 256u + 128u * (unsigned)ps, d = lds0 + dst + 2048u * (unsigned)bw;
        dma16(RFL(d), srcW0, srdW, RFL(sw)); dma16(RFL(d + 1024), srcW1, srdW, RFL(sw));
    };
    auto dma_wdj = [&](int head, int gj, unsigned dst) { dma_wjob(head, 2 * gj, dst); dma_wjob(head, 2 * gj + 1, dst + 8192u); };
    unsigned tag;

    f32x4 oacc[2][9];
    if (isB) {
        bf16x8 xf[36];
                const unsigned vx = (unsigned)(32 * bw + (lane >> 3)) * 1152u + 16u * (lane & 7);
        const unsigned sx = (unsigned)(b * 128) * 1152u;
#define XLOAD(c) do { const int kk_ = (c) + rot; const unsigned so_ = sx + 128u * (unsigned)(kk_ >= 9 ? kk_ - 9 : kk_); \
            LD16(xf[4 * (c)], vx, srdX, RFL(so_)); LD16(xf[4 * (c) + 1], vx, srdX, RFL(so_ + 9216u)); LD16(xf[4 * (c) + 2], vx, srdX, RFL(so_ + 18432u)); LD16(xf[4 * (c) + 3], vx, srdX, RFL(so_ + 27648u)); } while (0)
        XLOAD(0); XLOAD(1); XLOAD(2); XLOAD(3);
        if (XWIN > 4) XLOAD(4); if (XWIN > 5) XLOAD(5); if (XWIN > 6) XLOAD(6); if (XWIN > 7) XLOAD(7); if (XWIN > 8) XLOAD(8);
        const unsigned xwe = lds0 + EX + 4096u * bw + (unsigned)(lane >> 3) * 128u + 16u * (unsigned)((lane & 7) ^ ((lane >> 4) & 7));
        const unsigned xwo = lds0 + EX + 4096u * bw + (unsigned)(8 + (lane >> 3)) * 128u + 16u * (unsigned)((lane & 7) ^ ((4 + (lane >> 4)) & 7));
        const unsigned xrd = lds0 + EX + 4096u * bw + (unsigned)r32 * 128u + 16u * (unsigned)(hi ^ ((r32 >> 1) & 7));
        const unsigned wb0 = lds0 + 1024u * hi + 64u * (0 + q4) + 16u * ((2 * blk + (p4 & 1)) ^ (2 * hi + 0)) + 8u * (p4 >> 1);
        const unsigned wb1 = lds0 + 1024u * hi + 64u * (4 + q4) + 16u * ((2 * blk + (p4 & 1)) ^ (2 * hi + 1)) + 8u * (p4 >> 1);
        const unsigned qex = EX + 4096u * bw + lo16;
        unsigned tch = 0;
#define TOUCH(vo, srd, so) asm volatile("buffer_load_dword %0, %1, %2, %3 offen" : "+v"(tch) : "v"(vo), "s"(srd), "s"(so) : "memory")
#define TOUCH_WQ() do { int l_ = lane < 36 ? lane : 35; asm volatile("" : "+v"(l_)); const unsigned so_ = (unsigned)(72 * b + 18 * bw) * 8192u + (unsigned)(h0 + 1) * 256u; \
            TOUCH((unsigned)(l_ >> 1) * 8192u + 128u * (l_ & 1), srdW, RFL(so_)); } while (0)
#define TOUCH_WO(r) do { int l_ = lane < 36 ? lane : 35; asm volatile("" : "+v"(l_)); const unsigned so_ = (unsigned)(128 * (h0 + (r)) + 16 * b + 4 * bw) * 1152u; \
            TOUCH((unsigned)l_ * 128u, srdWo, RFL(so_)); } while (0)
        f32x16 acc[2];
        bf16x8 ar[8];
#define XCONV(c) do { asm volatile("s_waitcnt vmcnt(%4)" : "+v"(xf[4 * (c)]), "+v"(xf[4 * (c) + 1]), "+v"(xf[4 * (c) + 2]), "+v"(xf[4 * (c) + 3]) : "i"(4 * ((((c) - 1 + XWIN < 8) ? (c) - 1 + XWIN : 8) - (c)) + ((c) + XWIN > 8 ? 1 : 0)) : "memory"); \
            *(LAS bf16x8*)(uintptr_t)(xwe) = xf[4 * (c)]; *(LAS bf16x8*)(uintptr_t)(xwo) = xf[4 * (c) + 1]; *(LAS bf16x8*)(uintptr_t)(xwe + 2048) = xf[4 * (c) + 2]; *(LAS bf16x8*)(uintptr_t)(xwo + 2048) = xf[4 * (c) + 3]; \
            asm volatile("" ::: "memory"); \
            _Pragma("unroll") for (int ks = 0; ks < 4; ++ks) xf[4 * (c) + ks] = ldsrd128((lds_cptr)(uintptr_t)(xrd ^ (32u * ks))); \
            asm volatile("" ::: "memory"); \
            if ((c) + XWIN <= 8) XLOAD(((c) + XWIN <= 8) ? (c) + XWIN : 0); if ((c) + XWIN == 8) TOUCH_WQ(); } while (0)
#define WFR(wp0, wp1, m) cat8(vtr((wp0) + 8192 * ((m) >> 3) + 2048 * (((m) >> 1) & 3) + 512 * ((m) & 1)), vtr((wp1) + 8192 * ((m) >> 3) + 2048 * (((m) >> 1) & 3) + 512 * ((m) & 1)))
#define WPTR(p0, p1, base) unsigned p0##a = wb0 + (base), p1##a = wb1 + (base); asm volatile("" : "+v"(p0##a), "+v"(p1##a)); const lds_cptr p0 = (lds_cptr)(uintptr_t)p0##a, p1 = (lds_cptr)(uintptr_t)p1##a
#define QMF(gj, m) do { constexpr int n_ = 2 * (gj) + ((m) >> 3), kc_ = n_ % 9, ks_ = ((m) >> 1) & 3, dl_ = (m) & 1; \
            if (kc_ == 0 && ks_ == 0) acc[dl_] = MFMA32(ar[(m) & 7], xf[4 * kc_ + ks_], (f32x16{})); else acc[dl_] = MFMA32(ar[(m) & 7], xf[4 * kc_ + ks_], acc[dl_]); \
            PIN(acc[dl_]); \
            if (kc_ == 8 && ((m) & 7) == 7) { _Pragma("unroll") for (int dl = 0; dl < 2; ++dl) _Pragma("unroll") for (int sg = 0; sg < 2; ++sg) \
                *(LAS bf16x8*)(LW + qex + 16384u * (n_ / 9) + (2 * dl + sg) * 1024) = pack8s(acc[dl], sg, CSC); } } while (0)
#define QSTREAM(gj, base, MID, HASNEXT, nbase) do { \
            { WPTR(c0, c1, base); \
              QMF(gj, 0); ar[6] = WFR(c0, c1, 6); SB(); QMF(gj, 1); ar[7] = WFR(c0, c1, 7); SB(); QMF(gj, 2); ar[0] = WFR(c0, c1, 8); SB(); QMF(gj, 3); ar[1] = WFR(c0, c1, 9); SB(); \
              QMF(gj, 4); ar[2] = WFR(c0, c1, 10); SB(); QMF(gj, 5); ar[3] = WFR(c0, c1, 11); SB(); QMF(gj, 6); ar[4] = WFR(c0, c1, 12); SB(); QMF(gj, 7); ar[5] = WFR(c0, c1, 13); SB(); \
              QMF(gj, 8); ar[6] = WFR(c0, c1, 14); SB(); QMF(gj, 9); ar[7] = WFR(c0, c1, 15); SB(); QMF(gj, 10); SB(); QMF(gj, 11); SB(); } \
            MID; SB(); \
            if (HASNEXT) { WPTR(n0, n1, nbase); \
              QMF(gj, 12); ar[0] = WFR(n0, n1, 0); ar[1] = WFR(n0, n1, 1); SB(); QMF(gj, 13); ar[2] = WFR(n0, n1, 2); ar[3] = WFR(n0, n1, 3); SB(); \
              QMF(gj, 14); SB(); QMF(gj, 15); ar[4] = WFR(n0, n1, 4); ar[5] = WFR(n0, n1, 5); SB(); } \
            else { QMF(gj, 12); SB(); QMF(gj, 13); SB(); QMF(gj, 14); SB(); QMF(gj, 15); SB(); } \
        } while (0)
#define QDJ(T, gj, base) do { WPTR(c0, c1, base); \
            dma_k((T) + 2); \
            _Pragma("unroll") for (int m = 0; m < 8; ++m) ar[m] = WFR(c0, c1, m); SB(); \
            QMF(gj, 0); QMF(gj, 1); QMF(gj, 2); QMF(gj, 3); QMF(gj, 4); QMF(gj, 5); QMF(gj, 6); QMF(gj, 7); SB(); \
            dma_v((T) + 1); \
            _Pragma("unroll") for (int m = 8; m < 16; ++m) ar[m & 7] = WFR(c0, c1, m); SB(); \
            QMF(gj, 8); QMF(gj, 9); QMF(gj, 10); QMF(gj, 11); QMF(gj, 12); QMF(gj, 13); QMF(gj, 14); QMF(gj, 15); SB(); } while (0)
                XCONV(0); XCONV(1);
        LBAR();         { WPTR(c0, c1, 0u); ar[0] = WFR(c0, c1, 0); ar[1] = WFR(c0, c1, 1); ar[2] = WFR(c0, c1, 2); ar[3] = WFR(c0, c1, 3); ar[4] = WFR(c0, c1, 4); ar[5] = WFR(c0, c1, 5); }
        QSTREAM(0, 0u, LBAR(), true, SLOT);     XCONV(2); XCONV(3);         QSTREAM(1, SLOT, LBAR(), true, 3 * SLOT);          XCONV(4); XCONV(5);
        QSTREAM(2, 3 * SLOT, LBAR(), true, 2 * SLOT);      XCONV(6); XCONV(7);
        QSTREAM(3, 2 * SLOT, LBAR(), true, 4 * SLOT);      XCONV(8);
        QSTREAM(4, 4 * SLOT, LBAR(), true, 5 * SLOT);         QSTREAM(5, 5 * SLOT, LBAR(), true, EX + SLOT);
        QSTREAM(6, EX + SLOT, LBAR(), true, WR);
        QSTREAM(7, WR, LBAR(), true, WR + SLOT);
        QSTREAM(8, WR + SLOT, {}, false, 0u);
                LBAR();
#define BKV(T) do { if ((T) < 30) dma_k((T) + 2); if ((T) < 31) dma_v((T) + 1); } while (0)
        BKV(0); dma_wdj(h0 + 1, 0, WR); dma_wdj(h0 + 1, 1, WR + SLOT); WAITBAR(16);
        BKV(1); ELOAD(); WAITBAR(9);
        QDJ(2, 0, WR); WAITBAR(8);
        QDJ(3, 1, WR + SLOT); ETAG(); dma_wdj(h0 + 1, 2, WR); WAITBAR(12);
        BKV(4); dma_wdj(h0 + 1, 3, WR + SLOT); WAITBAR(12);
        QDJ(5, 2, WR); WAITBAR(8);
        QDJ(6, 3, WR + SLOT); dma_wdj(h0 + 1, 4, WR); WAITBAR(12);
        BKV(7); dma_wdj(h0 + 1, 5, WR + SLOT); WAITBAR(12);
        QDJ(8, 4, WR); WAITBAR(8);
        QDJ(9, 5, WR + SLOT); dma_wdj(h0 + 1, 6, WR); WAITBAR(12);
        BKV(10); dma_wdj(h0 + 1, 7, WR + SLOT); WAITBAR(12);
        QDJ(11, 6, WR); WAITBAR(8);
        QDJ(12, 7, WR + SLOT); dma_wdj(h0 + 1, 8, WR); WAITBAR(12);
        BKV(13); TOUCH_WO(0); TOUCH_WO(1); WAITBAR(10);
        QDJ(14, 8, WR); WAITBAR(8);
        BKV(15); WAITBAR(8);
        {
            int ln = lane; asm volatile("" : "+v"(ln));
            unsigned wov[5];
#pragma unroll
            for (int i = 0; i < 5; ++i) {
                const int pc = (i < 4) ? bw + 4 * i : 16 + (bw & 1);
                const int uu = 64 * pc + ln, pos = uu / 18, ch = uu - 18 * pos;
                const int k = (pos & ~12) | ((pos & 4) << 1) | ((pos & 8) >> 1);
                wov[i] = (unsigned)k * 1152u + 16u * ch;
            }
            const unsigned ab0 = lds0 + (unsigned)(16 * ((ln >> 4) >> 1) + 4 * ((ln >> 4) & 1) + ((ln & 15) >> 2)) * 288u + 8u * (ln & 3);
            auto dma_wo = [&](int r, int jj, unsigned dst) {
                const int hd = 8 * hq + 2 * ((c4 + (jj >> 1)) & 3) + r;
                const unsigned so = (unsigned)(128 * hd + 64 * (jj & 1)) * 1152u + 288u * c4;
#pragma unroll
                for (int i = 0; i < 5; ++i) { const int pc = (i < 4) ? bw + 4 * i : 16 + (bw & 1); dma16(RFL(lds0 + dst + 1024u * pc), wov[i], srdWo, RFL(so)); }
            };
#pragma unroll
            for (int i = 0; i < 2; ++i)
#pragma unroll
                for (int c = 0; c < 9; ++c) oacc[i][c] = f32x4{};
            bf16x8 ofx[4], ofy[4];
            unsigned flagv;
            const unsigned fo1 = WS_FO + (unsigned)((((g * 4 + hq) * 2 + 0) * 4 + bw) * 4 + (ln & 3)) * 4u;
#define O1L(OF, jj) do { const int hd_ = 8 * hq + 2 * ((c4 + ((jj) >> 1)) & 3); const unsigned so_ = (unsigned)((((g * 32 + hd_) * 8 + 2 * bw) * 4) + 2 * ((jj) & 1)) * 1024u; \
                LD16_SC1(OF[0], lo16, srdO, RFL(so_)); LD16_SC1(OF[1], lo16, srdO, RFL(so_ + 4096u)); LD16_SC1(OF[2], lo16, srdO, RFL(so_ + 1024u)); LD16_SC1(OF[3], lo16, srdO, RFL(so_ + 5120u)); } while (0)
#define OPHALF(base, OF, ks) do { \
                unsigned a0a = ab0 + (base) + 9216u * (ks); asm volatile("" : "+v"(a0a)); \
                const lds_cptr kb_ = (lds_cptr)(uintptr_t)a0a; bf16x8 af_[9]; \
                _Pragma("unroll") for (int c = 0; c < 9; ++c) af_[c] = cat8(vtr(kb_ + 32 * c), vtr(kb_ + 2304 + 32 * c)); \
                SB(); \
                _Pragma("unroll") for (int c = 0; c < 9; ++c) { oacc[0][c] = MFMA16(af_[c], OF[2 * (ks)], oacc[0][c]); oacc[1][c] = MFMA16(af_[c], OF[2 * (ks) + 1], oacc[1][c]); PIN(oacc[0][c]); PIN(oacc[1][c]); } \
                SB(); \
            } while (0)
#define WSL(jj) ((jj) % 3 == 0 ? WS2 : (jj) % 3 == 1 ? WS0 : WS1)
#define ISSUE1(jj, OF) do { dma_wo(0, jj, WSL(jj)); O1L(OF, jj); } while (0)
#define BKVC(T, jj, OF) do { asm volatile("" : "+v"(OF[0]), "+v"(OF[1]), "+v"(OF[2]), "+v"(OF[3]) :: "memory"); \
                if ((T) < 30) dma_k((T) + 2); OPHALF(WSL(jj), OF, 0); if ((T) < 31) dma_v((T) + 1); OPHALF(WSL(jj), OF, 1); } while (0)
            BKV(16); WAITBAR(8);
            {
                unsigned oa = lds0 + EX + 4096u * bw + lo16; asm volatile("" : "+v"(oa)); const lds_cptr op = (lds_cptr)(uintptr_t)oa;
                ofx[0] = ldsrd128(op); ofx[1] = ldsrd128(op + 16384); ofx[2] = ldsrd128(op + 1024); ofx[3] = ldsrd128(op + 17408);
                ofy[0] = ldsrd128(op + 2048); ofy[1] = ldsrd128(op + 18432); ofy[2] = ldsrd128(op + 3072); ofy[3] = ldsrd128(op + 19456); }
            BKV(17); dma_wo(0, 0, WSL(0));
            {   const unsigned so_ = (unsigned)((((g * 32 + h0) * 8 + 2 * bw) * 4)) * 1024u + lo16;
#pragma unroll
                for (int i = 0; i < 4; ++i) { st16_sc1(BCU(ofx[i]), so_ + 4096u * (i & 1) + 1024u * (i >> 1), srdO); st16_sc1(BCU(ofy[i]), so_ + 4096u * (i & 1) + 1024u * (2 + (i >> 1)), srdO); } }
            WAITBAR(21);
            BKV(18); dma_wo(0, 1, WSL(1)); WAITBAR(13);
            if (ln == 0) st4_sc1(tag, WS_FO + (unsigned)((((g * 4 + hq) * 2 + 0) * 4 + bw) * 4 + c4) * 4u, srdF);
            BKVC(19, 0, ofx); WAITBAR(8);
            BKVC(20, 1, ofy); WAITBAR(8);
            LD4_SC1(flagv, fo1, srdF);
            BKV(21); WAITBAR(8);
            BKV(22);
            asm volatile("" : "+v"(flagv) :: "memory");
            if (!__all(flagv == tag)) wait_tag(fo1, srdF, tag);
            ISSUE1(2, ofx); WB17; asm volatile("" :: "v"(tch));
            BKV(23); ISSUE1(3, ofy); WB17;
            BKVC(24, 2, ofx); ISSUE1(4, ofx); WB17;
            BKVC(25, 3, ofy); ISSUE1(5, ofy); WB17;
            BKVC(26, 4, ofx); ISSUE1(6, ofx); WB17;
            BKVC(27, 5, ofy); ISSUE1(7, ofy); WB17;
            BKVC(28, 6, ofx); dma_wo(1, 0, WS1); WAITBAR(13);
            BKVC(29, 7, ofy); dma_wo(1, 1, WS2); WAITBAR(13);
            BKV(30); dma_wo(1, 2, WS0); WAITBAR(9);
            BKV(31); dma_wo(1, 3, WS3); WAITBAR(5);
                                    WAITBAR(0);
            dma_wo(1, 4, 0u); dma_wo(1, 5, WCH); dma_wo(1, 6, 2u * WCH);
            dma_wo(1, 7, 3u * WCH);
        }
    } else {
        {
            constexpr unsigned PS[9] = {0u, SLOT, 3 * SLOT, 2 * SLOT, 4 * SLOT, 5 * SLOT, EX + SLOT, WR, WR + SLOT};
#pragma unroll
            for (int s = 0; s < WWIN; ++s) dma_wdj(h0, s, PS[s]);
            #define PSTEP(s) do { waitbar<4 * (((s) - 1 + WWIN < 8 ? (s) - 1 + WWIN : 8) - (s)) + ((s) == 7 ? 4 : (s) == 8 ? 12 : 0)>(); \
                if ((s) + WWIN <= 8) dma_wdj(h0, ((s) + WWIN <= 8) ? (s) + WWIN : 0, PS[((s) + WWIN <= 8) ? (s) + WWIN : 0]); } while (0)
            PSTEP(0); PSTEP(1); PSTEP(2); PSTEP(3); PSTEP(4); PSTEP(5);
            PSTEP(6); dma_k(0);
            PSTEP(7); dma_k(1); dma_v(0);
            PSTEP(8);
            WAITBAR(8);
            ELOAD();
        }
        const int qb = w;
        bf16x8 qf[8];
        { unsigned qa = lds0 + EX + 4096u * qb + lo16; asm volatile("" : "+v"(qa));
#pragma unroll
          for (int s = 0; s < 8; ++s) qf[s] = ldsrd128((lds_cptr)(uintptr_t)qa + 16384 * (s >> 2) + 1024 * (s & 3)); }
        const unsigned kb0 = 2048u * (r32 >> 3) + 64u * (r32 & 7) + 16u * ((0 + hi) ^ ((r32 >> 2) & 3));
        const unsigned kb1 = 2048u * (r32 >> 3) + 64u * (r32 & 7) + 16u * ((2 + hi) ^ ((r32 >> 2) & 3));
        const unsigned vb0 = 64u * (4 * hi + q4) + 16u * ((2 * blk + (p4 & 1)) ^ (0 + hi)) + 8u * (p4 >> 1);
        const unsigned vb1 = 2048u + 64u * (4 * hi + q4) + 16u * ((2 * blk + (p4 & 1)) ^ (2 + hi)) + 8u * (p4 >> 1);
        f32x16 o[4];
#pragma unroll
        for (int i = 0; i < 4; ++i) o[i] = f32x16{};
        f32x2 lsum = {0.f, 0.f};
        float fpend = 1.f; bool pend = false;
        f32x16 sA = f32x16{}, sB = f32x16{}, negm = f32x16{};
        u32x4 pA0 = {}, pA1 = {}, pB0 = {}, pB1 = {};
        bf16x8 vf0 = {}, vf1 = {}, vf2 = {}, vf3 = {};
        const unsigned kbb0 = lds0 + kb0, kbb1 = lds0 + kb1, vbb0 = lds0 + vb0 + 3 * SLOT, vbb1 = lds0 + vb1 + 3 * SLOT;
        unsigned scur = 0, sprev = 2 * SLOT;
#define KRD(kp, ks) ldsrd128((((ks) & 1) ? k1p : k0p) + (kp) + 512 * ((ks) >> 1))
#define VRDX(a0, a1, vp, sg, db) cat8(vtr((a0) + (vp) + 4096 * (sg) + 512 * (db)), vtr((a1) + (vp) + 4096 * (sg) + 512 * (db)))
#define VRD(vp, sg, db) VRDX(v0p, v1p, vp, sg, db)
#define BASES() asm volatile("" : "+s"(scur), "+s"(sprev)); \
            unsigned k0a = kbb0 + scur, k1a = kbb1 + scur, v0a = vbb0 + sprev, v1a = vbb1 + sprev, n0a = vbb0 + scur, n1a = vbb1 + scur; \
            asm volatile("" : "+v"(k0a), "+v"(k1a), "+v"(v0a), "+v"(v1a), "+v"(n0a), "+v"(n1a)); \
            const lds_cptr k0p = (lds_cptr)(uintptr_t)k0a, k1p = (lds_cptr)(uintptr_t)k1a, v0p = (lds_cptr)(uintptr_t)v0a, v1p = (lds_cptr)(uintptr_t)v1a, n0p = (lds_cptr)(uintptr_t)n0a, n1p = (lds_cptr)(uintptr_t)n1a
#define ADVANCE() do { sprev = scur; scur = (scur == 2 * SLOT) ? 0u : scur + SLOT; } while (0)
#define EX1(S, i) do { S[i] = __builtin_amdgcn_exp2f(S[i]); } while (0)
#define MX3(a, b, c) __builtin_fmaxf(__builtin_fmaxf(a, b), c)
#define AMAX3(d, a, b, c) asm volatile("v_max3_f32 %0, %1, %2, %3" : "=v"(d) : "v"(a), "v"(b), "v"(c))
#define AMAX2(d, a, b) asm volatile("v_max_f32 %0, %1, %2" : "=v"(d) : "v"(a), "v"(b))
#define PAIR(S, i) (f32x2{S[2 * (i)], S[2 * (i) + 1]})
#define RMAX1(S) ma_ = MX3(S[0], S[1], S[2]); mb_ = MX3(S[3], S[4], S[5]); mc_ = MX3(S[6], S[7], S[8]); md_ = MX3(S[9], S[10], S[11]); me_ = MX3(S[12], S[13], S[14]); \
            ma_ = MX3(ma_, mb_, mc_); md_ = MX3(md_, me_, S[15]); ma_ = MX3(ma_, md_, 0.f); PIN(ma_)
#define RMAX2() do { auto rr_ = __builtin_amdgcn_permlane32_swap(__float_as_uint(ma_), __float_as_uint(ma_), false, false); \
            rm_ = MX3(__uint_as_float(rr_[0]), __uint_as_float(rr_[1]), 0.f); PIN(rm_); } while (0)
#define APPLY_PEND() do { if (__builtin_expect(pend, 0)) { _Pragma("unroll") for (int d = 0; d < 4; ++d) _Pragma("unroll") for (int i = 0; i < 16; ++i) o[d][i] *= fpend; pend = false; } } while (0)
#define DECIDE(SN, PP1, DOPV) do { \
            if (__builtin_expect(__any(rm_ > THR), 0)) { const float dl_ = rm_; \
                _Pragma("unroll") for (int i = 0; i < 16; ++i) { SN[i] -= dl_; negm[i] -= dl_; } \
                fpend = __builtin_amdgcn_exp2f(-dl_); lsum *= fpend; pend = true; } \
        } while (0)
#define SLOTM(SC, SN, PP0, PP1, PC0, PC1, kp, vp, DOQK, DOPV, MID) do { \
            float rm_, ma_, mb_, mc_, md_, me_; f32x2 s0_, s1_; \
            bf16x8 k0_, k1_, k2_, k3_; \
            if (DOQK) { k1_ = KRD(kp, 1); k0_ = KRD(kp, 0); k3_ = KRD(kp, 3); k2_ = KRD(kp, 2); } \
            SB(); \
            APPLY_PEND(); \
              if (DOPV) o[0] = MFMA32(vf0, BC8(PP0), o[0]); RMAX1(SC); SB(); \
              if (DOPV) { o[1] = MFMA32(vf1, BC8(PP0), o[1]); vf1 = VRD(vp, 1, 1); vf0 = VRD(vp, 1, 0); } RMAX2(); SB(); \
              if (DOPV) o[2] = MFMA32(vf2, BC8(PP0), o[2]); SB(); \
              if (DOPV) { o[3] = MFMA32(vf3, BC8(PP0), o[3]); vf3 = VRD(vp, 1, 3); vf2 = VRD(vp, 1, 2); } SB(); \
            DECIDE(SC, PP1, DOPV); SB(); \
              if (DOQK) SN = MFMA32(k0_, qf[0], negm); EX1(SC, 0); EX1(SC, 1); PIN(SC); SB(); \
              if (DOPV) o[0] = MFMA32(vf0, BC8(PP1), o[0]); EX1(SC, 2); EX1(SC, 3); PC0[0] = cvtpk(SC[0], SC[1]); PIN(SC); PIN(PC0); SB(); \
              if (DOQK) { SN = MFMA32(k1_, qf[1], SN); k1_ = KRD(kp, 5); k0_ = KRD(kp, 4); } s0_ = PAIR(SC, 0) + PAIR(SC, 1); PIN2(s0_); EX1(SC, 4); EX1(SC, 5); PIN(SC); SB(); \
              if (DOPV) o[1] = MFMA32(vf1, BC8(PP1), o[1]); PC0[1] = cvtpk(SC[2], SC[3]); EX1(SC, 6); PC0[2] = cvtpk(SC[4], SC[5]); PIN(SC); PIN(PC0); SB(); \
              if (DOQK) SN = MFMA32(k2_, qf[2], SN); s0_ += PAIR(SC, 2); PIN2(s0_); EX1(SC, 7); EX1(SC, 8); PIN(SC); SB(); \
              if (DOPV) o[2] = MFMA32(vf2, BC8(PP1), o[2]); EX1(SC, 9); PC0[3] = cvtpk(SC[6], SC[7]); PIN(SC); PIN(PC0); SB(); \
              if (DOQK) { SN = MFMA32(k3_, qf[3], SN); k3_ = KRD(kp, 7); k2_ = KRD(kp, 6); } s1_ = PAIR(SC, 3) + PAIR(SC, 4); PIN2(s1_); EX1(SC, 10); EX1(SC, 11); PIN(SC); SB(); \
              if (DOPV) o[3] = MFMA32(vf3, BC8(PP1), o[3]); PC1[0] = cvtpk(SC[8], SC[9]); EX1(SC, 12); PC1[1] = cvtpk(SC[10], SC[11]); PIN(SC); PIN(PC1); SB(); \
              if (DOQK) SN = MFMA32(k0_, qf[4], SN); s1_ += PAIR(SC, 5); PIN2(s1_); EX1(SC, 13); EX1(SC, 14); PIN(SC); SB(); \
              if (DOQK) SN = MFMA32(k1_, qf[5], SN); EX1(SC, 15); PC1[2] = cvtpk(SC[12], SC[13]); PIN(SC); PIN(PC1); SB(); \
            MID; SB(); \
              if (DOQK) SN = MFMA32(k2_, qf[6], SN); s0_ += PAIR(SC, 6); s1_ += PAIR(SC, 7); PIN2(s0_); PIN2(s1_); PC1[3] = cvtpk(SC[14], SC[15]); PIN(PC1); SB(); \
              if (DOQK) SN = MFMA32(k3_, qf[7], SN); lsum += s0_; lsum += s1_; PIN2(lsum); SB(); \
        } while (0)
#define VPRE(a0, a1, vp) do { vf1 = VRDX(a0, a1, vp, 0, 1); vf0 = VRDX(a0, a1, vp, 0, 0); vf3 = VRDX(a0, a1, vp, 0, 3); vf2 = VRDX(a0, a1, vp, 0, 2); } while (0)
#define ABAR(Tn) do { LBAR(); } while (0)
        #pragma unroll 1
        for (int r = 0; r < 2; ++r) {
            {
                BASES();
#pragma unroll
                for (int ks = 0; ks < 8; ++ks) sA = MFMA32(KRD(0, ks), qf[ks], ks ? sA : (f32x16{}));
                float rm; { float a_ = MX3(sA[0], sA[1], sA[2]), b_ = MX3(sA[3], sA[4], sA[5]); a_ = MX3(a_, sA[6], sA[7]); b_ = MX3(b_, sA[8], sA[9]); a_ = MX3(a_, sA[10], sA[11]); b_ = MX3(b_, sA[12], sA[13]);
                    a_ = MX3(a_, sA[14], sA[15]); rm = swap32max(__builtin_fmaxf(a_, b_)); }
#pragma unroll
                for (int i = 0; i < 16; ++i) { sA[i] -= rm; negm[i] = -rm; }
                pend = false;
                SLOTM(sA, sB, pB0, pB1, pA0, pA1, 8192, 0, true, false, { if (r == 0) asm volatile("s_waitcnt vmcnt(0)" ::: "memory");
                    ABAR(16 * r + 1); VPRE(n0p, n1p, 0); });
                ADVANCE();
            }
#pragma unroll 1
            for (int t = 1; t < 16; ++t) {
                BASES();
                SLOTM(sB, sA, pA0, pA1, pB0, pB1, 0, 0, true, true, { VPRE(v0p, v1p, 8192); });
                SLOTM(sA, sB, pB0, pB1, pA0, pA1, 8192, 8192, true, true, { ABAR(16 * r + t + 1); VPRE(n0p, n1p, 0); });
                ADVANCE();
            }
            {
                BASES();
                SLOTM(sB, sA, pA0, pA1, pB0, pB1, 0, 0, false, true, { VPRE(v0p, v1p, 8192); });
                APPLY_PEND();
                o[0] = MFMA32(vf0, BC8(pB0), o[0]); o[1] = MFMA32(vf1, BC8(pB0), o[1]); vf1 = VRD(8192, 1, 1); vf0 = VRD(8192, 1, 0);
                o[2] = MFMA32(vf2, BC8(pB0), o[2]); o[3] = MFMA32(vf3, BC8(pB0), o[3]); vf3 = VRD(8192, 1, 3); vf2 = VRD(8192, 1, 2);
                o[0] = MFMA32(vf0, BC8(pB1), o[0]); o[1] = MFMA32(vf1, BC8(pB1), o[1]); o[2] = MFMA32(vf2, BC8(pB1), o[2]); o[3] = MFMA32(vf3, BC8(pB1), o[3]);
                if (r == 0) {
                    ETAG();
                    unsigned qa = lds0 + EX + 4096u * qb + lo16; asm volatile("" : "+v"(qa));
#pragma unroll
                    for (int s = 0; s < 8; ++s) qf[s] = ldsrd128((lds_cptr)(uintptr_t)qa + 16384 * (s >> 2) + 1024 * (s & 3));
                }
                const float inv = 1.0f / swap32sum(lsum[0] + lsum[1]);
                int ln = lane; asm volatile("" : "+v"(ln));
                const unsigned ovo = (unsigned)((ln >> 4) & 1) * 4096u + (unsigned)(ln & 15) * 16u + (unsigned)(ln >> 5) * 256u
                                   + (unsigned)((((g * 32 + h0 + r) * 8 + 2 * qb) * 4)) * 1024u;
                unsigned lo_ = lds0 + EX + 4096u * qb + (unsigned)((ln >> 4) & 1) * 16384u + (unsigned)(ln & 15) * 16u + (unsigned)(ln >> 5) * 256u; asm volatile("" : "+v"(lo_));
#pragma unroll
                for (int db = 0; db < 4; ++db)
#pragma unroll
                    for (int sg = 0; sg < 2; ++sg) { const bf16x8 pk_ = pack8s(o[db], sg, inv);
                        if (r == 0) *(LAS bf16x8*)(uintptr_t)(lo_ + 1024u * db + 512u * sg) = pk_; else st16_sc1(BCU(pk_), ovo + 1024u * db + 512u * sg, srdO); }
#pragma unroll
                for (int i = 0; i < 4; ++i) o[i] = f32x16{};
                lsum = f32x2{0.f, 0.f};
            }
        }
                LBAR();
#pragma unroll
        for (int i = 0; i < 2; ++i)
#pragma unroll
            for (int c = 0; c < 9; ++c) oacc[i][c] = f32x4{};
        asm volatile("s_waitcnt vmcnt(0)" ::: "memory");
        if (lane == 0) st4_sc1(tag, WS_FO + (unsigned)((((g * 4 + hq) * 2 + 1) * 4 + qb) * 4 + c4) * 4u, srdF);
    }
    {
        const int mg = w & 3, kq = w >> 2;
        int ln = lane; asm volatile("" : "+v"(ln));
        const unsigned ab0 = lds0 + (unsigned)(16 * ((ln >> 4) >> 1) + 4 * ((ln >> 4) & 1) + ((ln & 15) >> 2)) * 288u + 8u * (ln & 3);
        wait_tag(WS_FO + (unsigned)((((g * 4 + hq) * 2 + 1) * 4 + mg) * 4 + ((c4 + 2 * kq + (lane & 1)) & 3)) * 4u, srdF, tag);
                bf16x8 of[16];
#pragma unroll
        for (int i = 0; i < 8; ++i) {
            const int hd = 8 * hq + 2 * ((c4 + 2 * kq + (i >> 2)) & 3) + 1;
            const unsigned so = (unsigned)((((g * 32 + hd) * 8 + 2 * mg) * 4) + (i & 3)) * 1024u;
            LD16_SC1(of[2 * i], lo16, srdO, RFL(so)); LD16_SC1(of[2 * i + 1], lo16, srdO, RFL(so + 4096u));
        }
        asm volatile("s_waitcnt vmcnt(0)\n\ts_barrier" : "+v"(of[0]), "+v"(of[1]), "+v"(of[2]), "+v"(of[3]), "+v"(of[4]), "+v"(of[5]), "+v"(of[6]), "+v"(of[7]) :: "memory");
        asm volatile("" : "+v"(of[8]), "+v"(of[9]), "+v"(of[10]), "+v"(of[11]), "+v"(of[12]), "+v"(of[13]), "+v"(of[14]), "+v"(of[15]) :: "memory");
#pragma unroll
        for (int jj = 0; jj < 4; ++jj) {
            constexpr unsigned T0[4] = {WS1, WS2, WS0, WS3}, T1[4] = {0u, WCH, 2 * WCH, 3 * WCH};
            unsigned a0a = (kq ? T1[jj] : T0[jj]) + ab0; asm volatile("" : "+v"(a0a));
            const lds_cptr kb_ = (lds_cptr)(uintptr_t)a0a;
            bf16x8 af_[18];
#pragma unroll
            for (int ks = 0; ks < 2; ++ks)
#pragma unroll
                for (int c = 0; c < 9; ++c) af_[9 * ks + c] = cat8(vtr(kb_ + 9216 * ks + 32 * c), vtr(kb_ + 9216 * ks + 2304 + 32 * c));
            SB();
#pragma unroll
            for (int ks = 0; ks < 2; ++ks)
#pragma unroll
                for (int c = 0; c < 9; ++c) {
                    oacc[0][c] = MFMA16(af_[9 * ks + c], of[4 * jj + 2 * ks], oacc[0][c]);
                    oacc[1][c] = MFMA16(af_[9 * ks + c], of[4 * jj + 2 * ks + 1], oacc[1][c]);
                }
            SB();
        }
        LBAR();
        const unsigned xw = (unsigned)w * 9216u + lo16, xr = (unsigned)(w ^ 4) * 9216u + lo16;
#pragma unroll
        for (int c = 0; c < 9; ++c) *(LAS f32x4*)(LW + xw + 1024 * c) = kq ? oacc[0][c] : oacc[1][c];
        LBAR();
        f32x4 fin[9];
#pragma unroll
        for (int c = 0; c < 9; ++c) { const f32x4 t4 = *(const LAS f32x4*)(LW + xr + 1024 * c); fin[c] = (kq ? oacc[1][c] : oacc[0][c]) + t4; }
        const int mb = 2 * mg + kq;
        const unsigned lo8 = (unsigned)lane * 8u;
                if (mg != hq) {
            const unsigned sso = (unsigned)((((g * 4 + c4) * 8 + mb) * 4 + hq) * 9) * 512u + lo8;
#pragma unroll
            for (int c = 0; c < 9; ++c) { const u32x2 v2 = {cvtpk(fin[c][0], fin[c][1]), cvtpk(fin[c][2], fin[c][3])}; st8_sc1(v2, sso + 512u * c, srdS); }
            asm volatile("s_waitcnt vmcnt(0)" ::: "memory");
            if (lane == 0) st4_sc1(tag, WS_FS + (unsigned)(((g * 4 + c4) * 8 + mb) * 4 + hq) * 4u, srdF);
        } else {
            const int ot = (hq + 1 + (lane % 3)) & 3;
            wait_tag(WS_FS + (unsigned)(((g * 4 + c4) * 8 + mb) * 4 + ot) * 4u, srdF, tag);
            if (hq == 0 && c4 == 0 && kq == 0 && lane == 0) st4_sc1(tag, WS_E + 64u * (unsigned)g, srdF);
            u32x2 sv[27];
#pragma unroll
            for (int m = 0; m < 3; ++m)
#pragma unroll
                for (int c = 0; c < 9; ++c) LD8_SC1(sv[9 * m + c], lo8, srdS, RFL((unsigned)((((g * 4 + c4) * 8 + mb) * 4 + ((hq + 1 + m) & 3)) * 9 + c) * 512u));
            asm volatile("s_waitcnt vmcnt(0)" : "+v"(sv[0]), "+v"(sv[1]), "+v"(sv[2]), "+v"(sv[3]), "+v"(sv[4]), "+v"(sv[5]), "+v"(sv[6]), "+v"(sv[7]), "+v"(sv[8]) :: "memory");
            asm volatile("" : "+v"(sv[9]), "+v"(sv[10]), "+v"(sv[11]), "+v"(sv[12]), "+v"(sv[13]), "+v"(sv[14]), "+v"(sv[15]), "+v"(sv[16]), "+v"(sv[17]) :: "memory");
            asm volatile("" : "+v"(sv[18]), "+v"(sv[19]), "+v"(sv[20]), "+v"(sv[21]), "+v"(sv[22]), "+v"(sv[23]), "+v"(sv[24]), "+v"(sv[25]), "+v"(sv[26]) :: "memory");
#pragma unroll
            for (int c = 0; c < 9; ++c) {
                f32x4 a = fin[c];
#pragma unroll
                for (int m = 0; m < 3; ++m) { const u32x2 v = sv[9 * m + c];
                    a[0] += __uint_as_float(v[0] << 16); a[1] += __uint_as_float(v[0] & 0xFFFF0000u); a[2] += __uint_as_float(v[1] << 16); a[3] += __uint_as_float(v[1] & 0xFFFF0000u); }
                const u32x2 ov = {cvtpk(a[0], a[1]), cvtpk(a[2], a[3])};
                *(u32x2*)(out + (size_t)(b * 128 + 16 * mb + (lane & 15)) * 1152 + 576 * p + 144 * c4 + 16 * c + 4 * g4) = ov;
            }
        }
    }
    }

extern "C" void kernel_launch(void* const* d_in, const int* in_sizes, int n_in, void* d_out, int out_size, void* d_ws, size_t ws_size, hipStream_t stream) {
    (void)hipFuncSetAttribute((const void*)fused_kernel, hipFuncAttributeMaxDynamicSharedMemorySize, LDS_BYTES);
    hipLaunchKernelGGL(fused_kernel, dim3(256), dim3(512), LDS_BYTES, stream,
                       (const char*)d_in[0], (const char*)d_in[1], (const char*)d_in[2], (const char*)d_in[3], (const char*)d_in[4],
                       (const char*)d_in[5], (const char*)d_in[6], (const char*)d_in[7], (const char*)d_in[8], (const char*)d_in[9],
                       (char*)d_ws, (unsigned short*)d_out);
}
```

```cpp
#include <hip/hip_runtime.h>
#include <hip/hip_bf16.h>
#include <cstdint>

typedef short bf16x8 __attribute__((ext_vector_type(8)));
typedef short s16x4 __attribute__((ext_vector_type(4)));
typedef float f32x16 __attribute__((ext_vector_type(16)));
typedef float f32x4 __attribute__((ext_vector_type(4)));
typedef float f32x2 __attribute__((ext_vector_type(2)));
typedef __bf16 bf16x2_t __attribute__((ext_vector_type(2)));
typedef unsigned u32x4 __attribute__((ext_vector_type(4)));
typedef unsigned u32x2 __attribute__((ext_vector_type(2)));
#define LAS __attribute__((address_space(3)))
#define DI __device__ __forceinline__
typedef LAS const char* lds_cptr;
typedef LAS char* lds_ptr;

constexpr int KVBYTES = 8 * 32 * 1024 * 256;
constexpr unsigned SLOT = 16384;
constexpr int LDS_BYTES = 163840;
constexpr float CSC = 0.08838834764831845f * 1.4426950408889634f;
constexpr float THR = 8.0f;
constexpr unsigned EX = 6 * SLOT, WR = 8 * SLOT;
constexpr unsigned WCH = 18432u, WS0 = 108544u, WS1 = WS0 + WCH, WS2 = WS0 + 2 * WCH, WS3 = WS0 - WCH;
constexpr unsigned WS_E = 0, WS_FO = 4096, WS_FS = 16384;
constexpr size_t WS_O = 65536, WS_O_BYTES = (size_t)2048 * 4096 * 2;
constexpr size_t WS_SL = WS_O + WS_O_BYTES, WS_SL_BYTES = (size_t)16 * 4 * 8 * 4 * 9 * 512;
constexpr unsigned SPIN_CAP = 1u << 20;

#define MFMA32(a, b, c) __builtin_amdgcn_mfma_f32_32x32x16_bf16((a), (b), (c), 0, 0, 0)
#define MFMA16(a, b, c) __builtin_amdgcn_mfma_f32_16x16x32_bf16((a), (b), (c), 0, 0, 0)
#define RFL(x) __builtin_amdgcn_readfirstlane(x)
#define WAITBAR(N) asm volatile("s_waitcnt vmcnt(" #N ") lgkmcnt(0)\n\ts_barrier" ::: "memory")
template <int N> __device__ __forceinline__ void waitbar() { asm volatile("s_waitcnt vmcnt(%0) lgkmcnt(0)\n\ts_barrier" :: "i"(N) : "memory"); }
template <int N> __device__ __forceinline__ void waitv() { asm volatile("s_waitcnt vmcnt(%0)" :: "i"(N) : "memory"); }
#define WB17 WAITBAR(17)
#define WWIN 3
#define XWIN 4
#define LBAR() asm volatile("s_waitcnt lgkmcnt(0)\n\ts_barrier" ::: "memory")
#define SB() __builtin_amdgcn_sched_barrier(0)
#define PIN(x) asm volatile("" : "+v"(x))
#define PIN2(x) asm volatile("" : "+v"(x))

DI unsigned cvtpk(float lo, float hi) { f32x2 v = {lo, hi}; bf16x2_t b = __builtin_convertvector(v, bf16x2_t); return __builtin_bit_cast(unsigned, b); }
DI s16x4 vtr(lds_cptr p) { return __builtin_bit_cast(s16x4, __builtin_amdgcn_ds_read_tr16_b64_v4i16((LAS s16x4*)p)); }
DI bf16x8 cat8(s16x4 a, s16x4 b) { return __builtin_shufflevector(a, b, 0, 1, 2, 3, 4, 5, 6, 7); }
DI bf16x8 ldsrd128(lds_cptr p) { return *(const LAS bf16x8*)p; }
DI float swap32max(float m) { auto rr = __builtin_amdgcn_permlane32_swap(__float_as_uint(m), __float_as_uint(m), false, false); return __builtin_fmaxf(__uint_as_float(rr[0]), __uint_as_float(rr[1])); }
DI float swap32sum(float m) { auto rr = __builtin_amdgcn_permlane32_swap(__float_as_uint(m), __float_as_uint(m), false, false); return __uint_as_float(rr[0]) + __uint_as_float(rr[1]); }
DI bf16x8 pack8s(const f32x16& a, int sg, float sc) {
    u32x4 wv = {cvtpk(a[8 * sg + 0] * sc, a[8 * sg + 1] * sc), cvtpk(a[8 * sg + 2] * sc, a[8 * sg + 3] * sc), cvtpk(a[8 * sg + 4] * sc, a[8 * sg + 5] * sc), cvtpk(a[8 * sg + 6] * sc, a[8 * sg + 7] * sc)};
    return __builtin_bit_cast(bf16x8, wv);
}
#define BC8(x) __builtin_bit_cast(bf16x8, x)
#define BCU(x) __builtin_bit_cast(u32x4, x)

DI void dma16_nt(unsigned ldsdst, unsigned voff, __amdgpu_buffer_rsrc_t srd, unsigned soff) {
    asm volatile("s_mov_b32 m0, %0\n\ts_nop 0\n\tbuffer_load_dwordx4 %1, %2, %3 offen nt lds" :: "s"(ldsdst), "v"(voff), "s"(srd), "s"(soff) : "memory");
}
DI void dma16(unsigned ldsdst, unsigned voff, __amdgpu_buffer_rsrc_t srd, unsigned soff) {
    asm volatile("s_mov_b32 m0, %0\n\ts_nop 0\n\tbuffer_load_dwordx4 %1, %2, %3 offen lds" :: "s"(ldsdst), "v"(voff), "s"(srd), "s"(soff) : "memory");
}
DI void st16_sc1(u32x4 v, unsigned voff, __amdgpu_buffer_rsrc_t srd) { __builtin_amdgcn_raw_buffer_store_b128(v, srd, (int)voff, 0, 16); }
DI void st8_sc1(u32x2 v, unsigned voff, __amdgpu_buffer_rsrc_t srd) { __builtin_amdgcn_raw_buffer_store_b64(v, srd, (int)voff, 0, 16); }
DI void st4_sc1(unsigned v, unsigned voff, __amdgpu_buffer_rsrc_t srd) { __builtin_amdgcn_raw_buffer_store_b32(v, srd, (int)voff, 0, 16); }
#define LD16_SC1(dst, voff, srd, soff) asm volatile("buffer_load_dwordx4 %0, %1, %2, %3 offen sc1" : "=v"(dst) : "v"(voff), "s"(srd), "s"(soff) : "memory")
#define LD8_SC1(dst, voff, srd, soff) asm volatile("buffer_load_dwordx2 %0, %1, %2, %3 offen sc1" : "=v"(dst) : "v"(voff), "s"(srd), "s"(soff) : "memory")
#define LD4_SC1(dst, voff, srd) asm volatile("buffer_load_dword %0, %1, %2, 0 offen sc1" : "=v"(dst) : "v"(voff), "s"(srd) : "memory")
#define LD16(dst, voff, srd, soff) asm volatile("buffer_load_dwordx4 %0, %1, %2, %3 offen" : "=v"(dst) : "v"(voff), "s"(srd), "s"(soff) : "memory")
DI void wait_tag(unsigned voff, __amdgpu_buffer_rsrc_t srd, unsigned tag) {
    for (unsigned sp = 0; sp < SPIN_CAP; ++sp) {
        unsigned f;
        asm volatile("buffer_load_dword %0, %1, %2, 0 offen sc1\n\ts_waitcnt vmcnt(0)" : "=v"(f) : "v"(voff), "s"(srd) : "memory");
        if (__all(f == tag)) break;
        __builtin_amdgcn_s_sleep(1);
    }
}

__global__ __launch_bounds__(512, 2) void fused_kernel(const char* __restrict__ xA, const char* __restrict__ kA, const char* __restrict__ vA, const char* __restrict__ wqA, const char* __restrict__ woA,
                                                      const char* __restrict__ xB, const char* __restrict__ kB, const char* __restrict__ vB, const char* __restrict__ wqB, const char* __restrict__ woB,
                                                      char* __restrict__ ws, unsigned short* __restrict__ out) {
    extern __shared__ __attribute__((aligned(16))) char lds[];
    const int tid = threadIdx.x, lane = tid & 63;
    const int w = RFL(tid >> 6);
    const int r32 = lane & 31, hi = lane >> 5;
    const int wg = blockIdx.x, xcd = wg & 7, idx = wg >> 3;
    const int p = xcd & 1, hq = xcd >> 1, c4 = idx & 3, b = idx >> 2;
    const int g = p * 8 + b;
    const int h0 = 8 * hq + 2 * c4;
    const unsigned lds0 = (unsigned)(uintptr_t)lds;
    const lds_cptr L = (lds_cptr)lds;
    const lds_ptr LW = (lds_ptr)lds;
        const __amdgpu_buffer_rsrc_t srdX = __builtin_amdgcn_make_buffer_rsrc((void*)(p ? xB : xA), (short)0, 8 * 128 * 576 * 2, 0x00020000);
    const __amdgpu_buffer_rsrc_t srdW = __builtin_amdgcn_make_buffer_rsrc((void*)(p ? wqB : wqA), (short)0, 576 * 4096 * 2, 0x00020000);
    const __amdgpu_buffer_rsrc_t srdK = __builtin_amdgcn_make_buffer_rsrc((void*)(p ? kB : kA), (short)0, KVBYTES, 0x00020000);
    const __amdgpu_buffer_rsrc_t srdV = __builtin_amdgcn_make_buffer_rsrc((void*)(p ? vB : vA), (short)0, KVBYTES, 0x00020000);
    const __amdgpu_buffer_rsrc_t srdWo = __builtin_amdgcn_make_buffer_rsrc((void*)(p ? woB : woA), (short)0, 4096 * 576 * 2, 0x00020000);
    const __amdgpu_buffer_rsrc_t srdO = __builtin_amdgcn_make_buffer_rsrc((void*)(ws + WS_O), (short)0, (int)WS_O_BYTES, 0x00020000);
    const __amdgpu_buffer_rsrc_t srdS = __builtin_amdgcn_make_buffer_rsrc((void*)(ws + WS_SL), (short)0, (int)WS_SL_BYTES, 0x00020000);
    const __amdgpu_buffer_rsrc_t srdF = __builtin_amdgcn_make_buffer_rsrc((void*)ws, (short)0, 65536, 0x00020000);

    const bool isB = w >= 4;
    const int bw = w & 3;
    const int r7 = (lane >> 2) & 7;
    const int blk = (lane >> 4) & 1, q4 = (lane & 15) >> 2, p4 = lane & 3, g4 = lane >> 4;
    const unsigned lo16 = (unsigned)lane * 16u;
    unsigned ev;
#define ELOAD() do { const unsigned eo_ = WS_E + 64u * (unsigned)g; asm volatile("buffer_load_dword %0, %1, %2, 0 offen sc1" : "=v"(ev) : "v"(eo_), "s"(srdF) : "memory"); } while (0)
#define ETAG() do { PIN(ev); tag = (unsigned)RFL(ev) * 0x9E3779B1u + 0x7F4A7C15u; } while (0)
    const unsigned chs0 = 16u * (4u * (unsigned)hi + (unsigned)((lane & 3) ^ ((2 * (2 * bw) + (r7 >> 2)) & 3)));
    const unsigned chs1 = 16u * (4u * (unsigned)hi + (unsigned)((lane & 3) ^ ((2 * (2 * bw + 1) + (r7 >> 2)) & 3)));
    const unsigned srcKV0 = (unsigned)(16 * bw + r7) * 256u + chs0, srcKV1 = (unsigned)(16 * bw + 8 + r7) * 256u + chs1;
    const unsigned srcW0 = (unsigned)(16 * bw + r7) * 8192u + chs0, srcW1 = (unsigned)(16 * bw + 8 + r7) * 8192u + chs1;
    auto kv_off = [&](int T) -> unsigned { return (unsigned)((b * 32 + h0) * 1024 + 64 * T) * 256u; };
    auto kslot = [&](int T) -> unsigned { return (unsigned)(T % 3) * SLOT; };
    auto vslot = [&](int T) -> unsigned { return (unsigned)(3 + T % 3) * SLOT; };
    auto dma_tile = [&](__amdgpu_buffer_rsrc_t srd, int T, unsigned slot) { const unsigned so = kv_off(T), d = lds0 + 4096u * (unsigned)bw + slot;
        dma16_nt(RFL(d), srcKV0, srd, RFL(so)); dma16_nt(RFL(d + 1024), srcKV0, srd, RFL(so + 128));
        dma16_nt(RFL(d + 2048), srcKV1, srd, RFL(so)); dma16_nt(RFL(d + 3072), srcKV1, srd, RFL(so + 128)); };
    auto dma_k = [&](int T) { dma_tile(srdK, T, kslot(T)); };
    auto dma_v = [&](int T) { dma_tile(srdV, T, vslot(T)); };
    const int rot = (b + 2 * c4) % 9;
    auto dma_wjob = [&](int head, int n, unsigned dst) {
        const int ps = n / 9, kc0 = n - 9 * ps + rot, kc = kc0 >= 9 ? kc0 - 9 : kc0;
        const unsigned sw = (unsigned)(64 * kc) * 8192u + (unsigned)head * 256u + 128u * (unsigned)ps, d = lds0 + dst + 2048u * (unsigned)bw;
        dma16(RFL(d), srcW0, srdW, RFL(sw)); dma16(RFL(d + 1024), srcW1, srdW, RFL(sw));
    };
    auto dma_wdj = [&](int head, int gj, unsigned dst) { dma_wjob(head, 2 * gj, dst); dma_wjob(head, 2 * gj + 1, dst + 8192u); };
    unsigned tag;

    bf16x8 pk2[8];
    f32x4 oacc[2][9];
    if (isB) {
        bf16x8 xf[36];
                const unsigned vx = (unsigned)(32 * bw + (lane >> 3)) * 1152u + 16u * (lane & 7);
        const unsigned sx = (unsigned)(b * 128) * 1152u;
#define XLOAD(c) do { const int kk_ = (c) + rot; const unsigned so_ = sx + 128u * (unsigned)(kk_ >= 9 ? kk_ - 9 : kk_); \
            LD16(xf[4 * (c)], vx, srdX, RFL(so_)); LD16(xf[4 * (c) + 1], vx, srdX, RFL(so_ + 9216u)); LD16(xf[4 * (c) + 2], vx, srdX, RFL(so_ + 18432u)); LD16(xf[4 * (c) + 3], vx, srdX, RFL(so_ + 27648u)); } while (0)
        XLOAD(0); XLOAD(1); XLOAD(2); XLOAD(3);
        if (XWIN > 4) XLOAD(4); if (XWIN > 5) XLOAD(5); if (XWIN > 6) XLOAD(6); if (XWIN > 7) XLOAD(7); if (XWIN > 8) XLOAD(8);
        const unsigned xwe = lds0 + EX + 4096u * bw + (unsigned)(lane >> 3) * 128u + 16u * (unsigned)((lane & 7) ^ ((lane >> 4) & 7));
        const unsigned xwo = lds0 + EX + 4096u * bw + (unsigned)(8 + (lane >> 3)) * 128u + 16u * (unsigned)((lane & 7) ^ ((4 + (lane >> 4)) & 7));
        const unsigned xrd = lds0 + EX + 4096u * bw + (unsigned)r32 * 128u + 16u * (unsigned)(hi ^ ((r32 >> 1) & 7));
        const unsigned wb0 = lds0 + 1024u * hi + 64u * (0 + q4) + 16u * ((2 * blk + (p4 & 1)) ^ (2 * hi + 0)) + 8u * (p4 >> 1);
        const unsigned wb1 = lds0 + 1024u * hi + 64u * (4 + q4) + 16u * ((2 * blk + (p4 & 1)) ^ (2 * hi + 1)) + 8u * (p4 >> 1);
        const unsigned qex = EX + 4096u * bw + lo16;
        unsigned tch = 0;
#define TOUCH(vo, srd, so) asm volatile("buffer_load_dword %0, %1, %2, %3 offen" : "+v"(tch) : "v"(vo), "s"(srd), "s"(so) : "memory")
#define TOUCH_WQ() do { int l_ = lane < 36 ? lane : 35; asm volatile("" : "+v"(l_)); const unsigned so_ = (unsigned)(72 * b + 18 * bw) * 8192u + (unsigned)(h0 + 1) * 256u; \
            TOUCH((unsigned)(l_ >> 1) * 8192u + 128u * (l_ & 1), srdW, RFL(so_)); } while (0)
#define TOUCH_WO(r) do { int l_ = lane < 36 ? lane : 35; asm volatile("" : "+v"(l_)); const unsigned so_ = (unsigned)(128 * (h0 + (r)) + 16 * b + 4 * bw) * 1152u; \
            TOUCH((unsigned)l_ * 128u, srdWo, RFL(so_)); } while (0)
        f32x16 acc[2];
        bf16x8 ar[8];
#define XCONV(c) do { asm volatile("s_waitcnt vmcnt(%4)" : "+v"(xf[4 * (c)]), "+v"(xf[4 * (c) + 1]), "+v"(xf[4 * (c) + 2]), "+v"(xf[4 * (c) + 3]) : "i"(4 * ((((c) - 1 + XWIN < 8) ? (c) - 1 + XWIN : 8) - (c)) + ((c) + XWIN > 8 ? 1 : 0)) : "memory"); \
            *(LAS bf16x8*)(uintptr_t)(xwe) = xf[4 * (c)]; *(LAS bf16x8*)(uintptr_t)(xwo) = xf[4 * (c) + 1]; *(LAS bf16x8*)(uintptr_t)(xwe + 2048) = xf[4 * (c) + 2]; *(LAS bf16x8*)(uintptr_t)(xwo + 2048) = xf[4 * (c) + 3]; \
            asm volatile("" ::: "memory"); \
            _Pragma("unroll") for (int ks = 0; ks < 4; ++ks) xf[4 * (c) + ks] = ldsrd128((lds_cptr)(uintptr_t)(xrd ^ (32u * ks))); \
            asm volatile("" ::: "memory"); \
            if ((c) + XWIN <= 8) XLOAD(((c) + XWIN <= 8) ? (c) + XWIN : 0); if ((c) + XWIN == 8) TOUCH_WQ(); } while (0)
#define WFR(wp0, wp1, m) cat8(vtr((wp0) + 8192 * ((m) >> 3) + 2048 * (((m) >> 1) & 3) + 512 * ((m) & 1)), vtr((wp1) + 8192 * ((m) >> 3) + 2048 * (((m) >> 1) & 3) + 512 * ((m) & 1)))
#define WPTR(p0, p1, base) unsigned p0##a = wb0 + (base), p1##a = wb1 + (base); asm volatile("" : "+v"(p0##a), "+v"(p1##a)); const lds_cptr p0 = (lds_cptr)(uintptr_t)p0##a, p1 = (lds_cptr)(uintptr_t)p1##a
#define QMF(gj, m) do { constexpr int n_ = 2 * (gj) + ((m) >> 3), kc_ = n_ % 9, ks_ = ((m) >> 1) & 3, dl_ = (m) & 1; \
            if (kc_ == 0 && ks_ == 0) acc[dl_] = MFMA32(ar[(m) & 7], xf[4 * kc_ + ks_], (f32x16{})); else acc[dl_] = MFMA32(ar[(m) & 7], xf[4 * kc_ + ks_], acc[dl_]); \
            PIN(acc[dl_]); \
            if (kc_ == 8 && ((m) & 7) == 7) { _Pragma("unroll") for (int dl = 0; dl < 2; ++dl) _Pragma("unroll") for (int sg = 0; sg < 2; ++sg) \
                *(LAS bf16x8*)(LW + qex + 16384u * (n_ / 9) + (2 * dl + sg) * 1024) = pack8s(acc[dl], sg, CSC); } } while (0)
#define QSTREAM(gj, base, MID, HASNEXT, nbase) do { \
            { WPTR(c0, c1, base); \
              QMF(gj, 0); ar[6] = WFR(c0, c1, 6); SB(); QMF(gj, 1); ar[7] = WFR(c0, c1, 7); SB(); QMF(gj, 2); ar[0] = WFR(c0, c1, 8); SB(); QMF(gj, 3); ar[1] = WFR(c0, c1, 9); SB(); \
              QMF(gj, 4); ar[2] = WFR(c0, c1, 10); SB(); QMF(gj, 5); ar[3] = WFR(c0, c1, 11); SB(); QMF(gj, 6); ar[4] = WFR(c0, c1, 12); SB(); QMF(gj, 7); ar[5] = WFR(c0, c1, 13); SB(); \
              QMF(gj, 8); ar[6] = WFR(c0, c1, 14); SB(); QMF(gj, 9); ar[7] = WFR(c0, c1, 15); SB(); QMF(gj, 10); SB(); QMF(gj, 11); SB(); } \
            MID; SB(); \
            if (HASNEXT) { WPTR(n0, n1, nbase); \
              QMF(gj, 12); ar[0] = WFR(n0, n1, 0); ar[1] = WFR(n0, n1, 1); SB(); QMF(gj, 13); ar[2] = WFR(n0, n1, 2); ar[3] = WFR(n0, n1, 3); SB(); \
              QMF(gj, 14); SB(); QMF(gj, 15); ar[4] = WFR(n0, n1, 4); ar[5] = WFR(n0, n1, 5); SB(); } \
            else { QMF(gj, 12); SB(); QMF(gj, 13); SB(); QMF(gj, 14); SB(); QMF(gj, 15); SB(); } \
        } while (0)
#define QDJ(T, gj, base) do { WPTR(c0, c1, base); \
            dma_k((T) + 2); \
            _Pragma("unroll") for (int m = 0; m < 8; ++m) ar[m] = WFR(c0, c1, m); SB(); \
            QMF(gj, 0); QMF(gj, 1); QMF(gj, 2); QMF(gj, 3); QMF(gj, 4); QMF(gj, 5); QMF(gj, 6); QMF(gj, 7); SB(); \
            dma_v((T) + 1); \
            _Pragma("unroll") for (int m = 8; m < 16; ++m) ar[m & 7] = WFR(c0, c1, m); SB(); \
            QMF(gj, 8); QMF(gj, 9); QMF(gj, 10); QMF(gj, 11); QMF(gj, 12); QMF(gj, 13); QMF(gj, 14); QMF(gj, 15); SB(); } while (0)
                XCONV(0); XCONV(1);
        LBAR();         { WPTR(c0, c1, 0u); ar[0] = WFR(c0, c1, 0); ar[1] = WFR(c0, c1, 1); ar[2] = WFR(c0, c1, 2); ar[3] = WFR(c0, c1, 3); ar[4] = WFR(c0, c1, 4); ar[5] = WFR(c0, c1, 5); }
        QSTREAM(0, 0u, LBAR(), true, SLOT);     XCONV(2); XCONV(3);         QSTREAM(1, SLOT, LBAR(), true, 3 * SLOT);          XCONV(4); XCONV(5);
        QSTREAM(2, 3 * SLOT, LBAR(), true, 2 * SLOT);      XCONV(6); XCONV(7);
        QSTREAM(3, 2 * SLOT, LBAR(), true, 4 * SLOT);      XCONV(8);
        QSTREAM(4, 4 * SLOT, LBAR(), true, 5 * SLOT);         QSTREAM(5, 5 * SLOT, LBAR(), true, EX + SLOT);
        QSTREAM(6, EX + SLOT, LBAR(), true, WR);
        QSTREAM(7, WR, LBAR(), true, WR + SLOT);
        QSTREAM(8, WR + SLOT, {}, false, 0u);
                LBAR();
#define BKV(T) do { if ((T) < 30) dma_k((T) + 2); if ((T) < 31) dma_v((T) + 1); } while (0)
        BKV(0); dma_wdj(h0 + 1, 0, WR); dma_wdj(h0 + 1, 1, WR + SLOT); WAITBAR(16);
        BKV(1); ELOAD(); WAITBAR(9);
        QDJ(2, 0, WR); WAITBAR(8);
        QDJ(3, 1, WR + SLOT); ETAG(); dma_wdj(h0 + 1, 2, WR); WAITBAR(12);
        BKV(4); dma_wdj(h0 + 1, 3, WR + SLOT); WAITBAR(12);
        QDJ(5, 2, WR); WAITBAR(8);
        QDJ(6, 3, WR + SLOT); dma_wdj(h0 + 1, 4, WR); WAITBAR(12);
        BKV(7); dma_wdj(h0 + 1, 5, WR + SLOT); WAITBAR(12);
        QDJ(8, 4, WR); WAITBAR(8);
        QDJ(9, 5, WR + SLOT); dma_wdj(h0 + 1, 6, WR); WAITBAR(12);
        BKV(10); dma_wdj(h0 + 1, 7, WR + SLOT); WAITBAR(12);
        QDJ(11, 6, WR); WAITBAR(8);
        QDJ(12, 7, WR + SLOT); dma_wdj(h0 + 1, 8, WR); WAITBAR(12);
        BKV(13); TOUCH_WO(0); TOUCH_WO(1); WAITBAR(10);
        QDJ(14, 8, WR); WAITBAR(8);
        BKV(15); WAITBAR(8);
        {
            int ln = lane; asm volatile("" : "+v"(ln));
            unsigned wov[5];
#pragma unroll
            for (int i = 0; i < 5; ++i) {
                const int pc = (i < 4) ? bw + 4 * i : 16 + (bw & 1);
                const int uu = 64 * pc + ln, pos = uu / 18, ch = uu - 18 * pos;
                const int k = (pos & ~12) | ((pos & 4) << 1) | ((pos & 8) >> 1);
                wov[i] = (unsigned)k * 1152u + 16u * ch;
            }
            const unsigned ab0 = lds0 + (unsigned)(16 * ((ln >> 4) >> 1) + 4 * ((ln >> 4) & 1) + ((ln & 15) >> 2)) * 288u + 8u * (ln & 3);
            auto dma_wo = [&](int r, int jj, unsigned dst) {
                const int hd = 8 * hq + 2 * ((c4 + (jj >> 1)) & 3) + r;
                const unsigned so = (unsigned)(128 * hd + 64 * (jj & 1)) * 1152u + 288u * c4;
#pragma unroll
                for (int i = 0; i < 5; ++i) { const int pc = (i < 4) ? bw + 4 * i : 16 + (bw & 1); dma16(RFL(lds0 + dst + 1024u * pc), wov[i], srdWo, RFL(so)); }
            };
#pragma unroll
            for (int i = 0; i < 2; ++i)
#pragma unroll
                for (int c = 0; c < 9; ++c) oacc[i][c] = f32x4{};
            bf16x8 ofx[4], ofy[4];
            unsigned flagv;
            const unsigned fo1 = WS_FO + (unsigned)((((g * 4 + hq) * 2 + 0) * 4 + bw) * 4 + (ln & 3)) * 4u;
#define O1L(OF, jj) do { const int hd_ = 8 * hq + 2 * ((c4 + ((jj) >> 1)) & 3); const unsigned so_ = (unsigned)((((g * 32 + hd_) * 8 + 2 * bw) * 4) + 2 * ((jj) & 1)) * 1024u; \
                LD16_SC1(OF[0], lo16, srdO, RFL(so_)); LD16_SC1(OF[1], lo16, srdO, RFL(so_ + 4096u)); LD16_SC1(OF[2], lo16, srdO, RFL(so_ + 1024u)); LD16_SC1(OF[3], lo16, srdO, RFL(so_ + 5120u)); } while (0)
#define OPHALF(base, OF, ks) do { \
                unsigned a0a = ab0 + (base) + 9216u * (ks); asm volatile("" : "+v"(a0a)); \
                const lds_cptr kb_ = (lds_cptr)(uintptr_t)a0a; bf16x8 af_[9]; \
                _Pragma("unroll") for (int c = 0; c < 9; ++c) af_[c] = cat8(vtr(kb_ + 32 * c), vtr(kb_ + 2304 + 32 * c)); \
                SB(); \
                _Pragma("unroll") for (int c = 0; c < 9; ++c) { oacc[0][c] = MFMA16(af_[c], OF[2 * (ks)], oacc[0][c]); oacc[1][c] = MFMA16(af_[c], OF[2 * (ks) + 1], oacc[1][c]); PIN(oacc[0][c]); PIN(oacc[1][c]); } \
                SB(); \
            } while (0)
#define WSL(jj) ((jj) % 3 == 0 ? WS2 : (jj) % 3 == 1 ? WS0 : WS1)
#define ISSUE1(jj, OF) do { dma_wo(0, jj, WSL(jj)); O1L(OF, jj); } while (0)
#define BKVC(T, jj, OF) do { asm volatile("" : "+v"(OF[0]), "+v"(OF[1]), "+v"(OF[2]), "+v"(OF[3]) :: "memory"); \
                if ((T) < 30) dma_k((T) + 2); OPHALF(WSL(jj), OF, 0); if ((T) < 31) dma_v((T) + 1); OPHALF(WSL(jj), OF, 1); } while (0)
            BKV(16); WAITBAR(8);
            {
                unsigned oa = lds0 + EX + 4096u * bw + lo16; asm volatile("" : "+v"(oa)); const lds_cptr op = (lds_cptr)(uintptr_t)oa;
                ofx[0] = ldsrd128(op); ofx[1] = ldsrd128(op + 16384); ofx[2] = ldsrd128(op + 1024); ofx[3] = ldsrd128(op + 17408);
                ofy[0] = ldsrd128(op + 2048); ofy[1] = ldsrd128(op + 18432); ofy[2] = ldsrd128(op + 3072); ofy[3] = ldsrd128(op + 19456); }
            BKV(17); dma_wo(0, 0, WSL(0));
            {   const unsigned so_ = (unsigned)((((g * 32 + h0) * 8 + 2 * bw) * 4)) * 1024u + lo16;
#pragma unroll
                for (int i = 0; i < 4; ++i) { st16_sc1(BCU(ofx[i]), so_ + 4096u * (i & 1) + 1024u * (i >> 1), srdO); st16_sc1(BCU(ofy[i]), so_ + 4096u * (i & 1) + 1024u * (2 + (i >> 1)), srdO); } }
            WAITBAR(21);
            BKV(18); dma_wo(0, 1, WSL(1)); WAITBAR(13);
            if (ln == 0) st4_sc1(tag, WS_FO + (unsigned)((((g * 4 + hq) * 2 + 0) * 4 + bw) * 4 + c4) * 4u, srdF);
            BKVC(19, 0, ofx); WAITBAR(8);
            BKVC(20, 1, ofy); WAITBAR(8);
            LD4_SC1(flagv, fo1, srdF);
            BKV(21); WAITBAR(8);
            BKV(22);
            asm volatile("" : "+v"(flagv) :: "memory");
            if (!__all(flagv == tag)) wait_tag(fo1, srdF, tag);
            ISSUE1(2, ofx); WB17; asm volatile("" :: "v"(tch));
            BKV(23); ISSUE1(3, ofy); WB17;
            BKVC(24, 2, ofx); ISSUE1(4, ofx); WB17;
            BKVC(25, 3, ofy); ISSUE1(5, ofy); WB17;
            BKVC(26, 4, ofx); ISSUE1(6, ofx); WB17;
            BKVC(27, 5, ofy); ISSUE1(7, ofy); WB17;
            BKVC(28, 6, ofx); dma_wo(1, 0, WS1); WAITBAR(13);
            BKVC(29, 7, ofy); dma_wo(1, 1, WS2); WAITBAR(13);
            BKV(30); dma_wo(1, 2, WS0); WAITBAR(9);
            BKV(31); dma_wo(1, 3, WS3); WAITBAR(5);
                                    WAITBAR(0);
            dma_wo(1, 4, 0u); dma_wo(1, 5, WCH); dma_wo(1, 6, 2u * WCH);
            dma_wo(1, 7, 3u * WCH);
        }
    } else {
        {
            constexpr unsigned PS[9] = {0u, SLOT, 3 * SLOT, 2 * SLOT, 4 * SLOT, 5 * SLOT, EX + SLOT, WR, WR + SLOT};
#pragma unroll
            for (int s = 0; s < WWIN; ++s) dma_wdj(h0, s, PS[s]);
            #define PSTEP(s) do { waitbar<4 * (((s) - 1 + WWIN < 8 ? (s) - 1 + WWIN : 8) - (s)) + ((s) == 7 ? 4 : (s) == 8 ? 12 : 0)>(); \
                if ((s) + WWIN <= 8) dma_wdj(h0, ((s) + WWIN <= 8) ? (s) + WWIN : 0, PS[((s) + WWIN <= 8) ? (s) + WWIN : 0]); } while (0)
            PSTEP(0); PSTEP(1); PSTEP(2); PSTEP(3); PSTEP(4); PSTEP(5);
            PSTEP(6); dma_k(0);
            PSTEP(7); dma_k(1); dma_v(0);
            PSTEP(8);
            WAITBAR(8);
            ELOAD();
        }
        const int qb = w;
        bf16x8 qf[8];
        { unsigned qa = lds0 + EX + 4096u * qb + lo16; asm volatile("" : "+v"(qa));
#pragma unroll
          for (int s = 0; s < 8; ++s) qf[s] = ldsrd128((lds_cptr)(uintptr_t)qa + 16384 * (s >> 2) + 1024 * (s & 3)); }
        const unsigned kb0 = 2048u * (r32 >> 3) + 64u * (r32 & 7) + 16u * ((0 + hi) ^ ((r32 >> 2) & 3));
        const unsigned kb1 = 2048u * (r32 >> 3) + 64u * (r32 & 7) + 16u * ((2 + hi) ^ ((r32 >> 2) & 3));
        const unsigned vb0 = 64u * (4 * hi + q4) + 16u * ((2 * blk + (p4 & 1)) ^ (0 + hi)) + 8u * (p4 >> 1);
        const unsigned vb1 = 2048u + 64u * (4 * hi + q4) + 16u * ((2 * blk + (p4 & 1)) ^ (2 + hi)) + 8u * (p4 >> 1);
        f32x16 o[4];
#pragma unroll
        for (int i = 0; i < 4; ++i) o[i] = f32x16{};
        f32x2 lsum = {0.f, 0.f};
        float fpend = 1.f; bool pend = false;
        f32x16 sA = f32x16{}, sB = f32x16{}, negm = f32x16{};
        u32x4 pA0 = {}, pA1 = {}, pB0 = {}, pB1 = {};
        bf16x8 vf0 = {}, vf1 = {}, vf2 = {}, vf3 = {};
        const unsigned kbb0 = lds0 + kb0, kbb1 = lds0 + kb1, vbb0 = lds0 + vb0 + 3 * SLOT, vbb1 = lds0 + vb1 + 3 * SLOT;
        unsigned scur = 0, sprev = 2 * SLOT;
#define KRD(kp, ks) ldsrd128((((ks) & 1) ? k1p : k0p) + (kp) + 512 * ((ks) >> 1))
#define VRDX(a0, a1, vp, sg, db) cat8(vtr((a0) + (vp) + 4096 * (sg) + 512 * (db)), vtr((a1) + (vp) + 4096 * (sg) + 512 * (db)))
#define VRD(vp, sg, db) VRDX(v0p, v1p, vp, sg, db)
#define BASES() asm volatile("" : "+s"(scur), "+s"(sprev)); \
            unsigned k0a = kbb0 + scur, k1a = kbb1 + scur, v0a = vbb0 + sprev, v1a = vbb1 + sprev, n0a = vbb0 + scur, n1a = vbb1 + scur; \
            asm volatile("" : "+v"(k0a), "+v"(k1a), "+v"(v0a), "+v"(v1a), "+v"(n0a), "+v"(n1a)); \
            const lds_cptr k0p = (lds_cptr)(uintptr_t)k0a, k1p = (lds_cptr)(uintptr_t)k1a, v0p = (lds_cptr)(uintptr_t)v0a, v1p = (lds_cptr)(uintptr_t)v1a, n0p = (lds_cptr)(uintptr_t)n0a, n1p = (lds_cptr)(uintptr_t)n1a
#define ADVANCE() do { sprev = scur; scur = (scur == 2 * SLOT) ? 0u : scur + SLOT; } while (0)
#define EX1(S, i) do { S[i] = __builtin_amdgcn_exp2f(S[i]); } while (0)
#define MX3(a, b, c) __builtin_fmaxf(__builtin_fmaxf(a, b), c)
#define AMAX3(d, a, b, c) asm volatile("v_max3_f32 %0, %1, %2, %3" : "=v"(d) : "v"(a), "v"(b), "v"(c))
#define AMAX2(d, a, b) asm volatile("v_max_f32 %0, %1, %2" : "=v"(d) : "v"(a), "v"(b))
#define PAIR(S, i) (f32x2{S[2 * (i)], S[2 * (i) + 1]})
#define RMAX1(S) ma_ = MX3(S[0], S[1], S[2]); mb_ = MX3(S[3], S[4], S[5]); mc_ = MX3(S[6], S[7], S[8]); md_ = MX3(S[9], S[10], S[11]); me_ = MX3(S[12], S[13], S[14]); \
            ma_ = MX3(ma_, mb_, mc_); md_ = MX3(md_, me_, S[15]); ma_ = MX3(ma_, md_, 0.f); PIN(ma_)
#define RMAX2() do { auto rr_ = __builtin_amdgcn_permlane32_swap(__float_as_uint(ma_), __float_as_uint(ma_), false, false); \
            rm_ = MX3(__uint_as_float(rr_[0]), __uint_as_float(rr_[1]), 0.f); PIN(rm_); } while (0)
#define APPLY_PEND() do { if (__builtin_expect(pend, 0)) { _Pragma("unroll") for (int d = 0; d < 4; ++d) _Pragma("unroll") for (int i = 0; i < 16; ++i) o[d][i] *= fpend; pend = false; } } while (0)
#define DECIDE(SN, PP1, DOPV) do { \
            if (__builtin_expect(__any(rm_ > THR), 0)) { const float dl_ = rm_; \
                _Pragma("unroll") for (int i = 0; i < 16; ++i) { SN[i] -= dl_; negm[i] -= dl_; } \
                fpend = __builtin_amdgcn_exp2f(-dl_); lsum *= fpend; pend = true; } \
        } while (0)
#define SLOTM(SC, SN, PP0, PP1, PC0, PC1, kp, vp, DOQK, DOPV, MID) do { \
            float rm_, ma_, mb_, mc_, md_, me_; f32x2 s0_, s1_; \
            bf16x8 k0_, k1_, k2_, k3_; \
            if (DOQK) { k1_ = KRD(kp, 1); k0_ = KRD(kp, 0); k3_ = KRD(kp, 3); k2_ = KRD(kp, 2); } \
            SB(); \
            APPLY_PEND(); \
              if (DOPV) o[0] = MFMA32(vf0, BC8(PP0), o[0]); RMAX1(SC); SB(); \
              if (DOPV) { o[1] = MFMA32(vf1, BC8(PP0), o[1]); vf1 = VRD(vp, 1, 1); vf0 = VRD(vp, 1, 0); } RMAX2(); SB(); \
              if (DOPV) o[2] = MFMA32(vf2, BC8(PP0), o[2]); SB(); \
              if (DOPV) { o[3] = MFMA32(vf3, BC8(PP0), o[3]); vf3 = VRD(vp, 1, 3); vf2 = VRD(vp, 1, 2); } SB(); \
            DECIDE(SC, PP1, DOPV); SB(); \
              if (DOQK) SN = MFMA32(k0_, qf[0], negm); EX1(SC, 0); EX1(SC, 1); PIN(SC); SB(); \
              if (DOPV) o[0] = MFMA32(vf0, BC8(PP1), o[0]); EX1(SC, 2); EX1(SC, 3); PC0[0] = cvtpk(SC[0], SC[1]); PIN(SC); PIN(PC0); SB(); \
              if (DOQK) { SN = MFMA32(k1_, qf[1], SN); k1_ = KRD(kp, 5); k0_ = KRD(kp, 4); } s0_ = PAIR(SC, 0) + PAIR(SC, 1); PIN2(s0_); EX1(SC, 4); EX1(SC, 5); PIN(SC); SB(); \
              if (DOPV) o[1] = MFMA32(vf1, BC8(PP1), o[1]); PC0[1] = cvtpk(SC[2], SC[3]); EX1(SC, 6); PC0[2] = cvtpk(SC[4], SC[5]); PIN(SC); PIN(PC0); SB(); \
              if (DOQK) SN = MFMA32(k2_, qf[2], SN); s0_ += PAIR(SC, 2); PIN2(s0_); EX1(SC, 7); EX1(SC, 8); PIN(SC); SB(); \
              if (DOPV) o[2] = MFMA32(vf2, BC8(PP1), o[2]); EX1(SC, 9); PC0[3] = cvtpk(SC[6], SC[7]); PIN(SC); PIN(PC0); SB(); \
              if (DOQK) { SN = MFMA32(k3_, qf[3], SN); k3_ = KRD(kp, 7); k2_ = KRD(kp, 6); } s1_ = PAIR(SC, 3) + PAIR(SC, 4); PIN2(s1_); EX1(SC, 10); EX1(SC, 11); PIN(SC); SB(); \
              if (DOPV) o[3] = MFMA32(vf3, BC8(PP1), o[3]); PC1[0] = cvtpk(SC[8], SC[9]); EX1(SC, 12); PC1[1] = cvtpk(SC[10], SC[11]); PIN(SC); PIN(PC1); SB(); \
              if (DOQK) SN = MFMA32(k0_, qf[4], SN); s1_ += PAIR(SC, 5); PIN2(s1_); EX1(SC, 13); EX1(SC, 14); PIN(SC); SB(); \
              if (DOQK) SN = MFMA32(k1_, qf[5], SN); EX1(SC, 15); PC1[2] = cvtpk(SC[12], SC[13]); PIN(SC); PIN(PC1); SB(); \
            MID; SB(); \
              if (DOQK) SN = MFMA32(k2_, qf[6], SN); s0_ += PAIR(SC, 6); s1_ += PAIR(SC, 7); PIN2(s0_); PIN2(s1_); PC1[3] = cvtpk(SC[14], SC[15]); PIN(PC1); SB(); \
              if (DOQK) SN = MFMA32(k3_, qf[7], SN); lsum += s0_; lsum += s1_; PIN2(lsum); SB(); \
        } while (0)
#define VPRE(a0, a1, vp) do { vf1 = VRDX(a0, a1, vp, 0, 1); vf0 = VRDX(a0, a1, vp, 0, 0); vf3 = VRDX(a0, a1, vp, 0, 3); vf2 = VRDX(a0, a1, vp, 0, 2); } while (0)
#define ABAR(Tn) do { LBAR(); } while (0)
        #pragma unroll 1
        for (int r = 0; r < 2; ++r) {
            {
                BASES();
#pragma unroll
                for (int ks = 0; ks < 8; ++ks) sA = MFMA32(KRD(0, ks), qf[ks], ks ? sA : (f32x16{}));
                float rm; { float a_ = MX3(sA[0], sA[1], sA[2]), b_ = MX3(sA[3], sA[4], sA[5]); a_ = MX3(a_, sA[6], sA[7]); b_ = MX3(b_, sA[8], sA[9]); a_ = MX3(a_, sA[10], sA[11]); b_ = MX3(b_, sA[12], sA[13]);
                    a_ = MX3(a_, sA[14], sA[15]); rm = swap32max(__builtin_fmaxf(a_, b_)); }
#pragma unroll
                for (int i = 0; i < 16; ++i) { sA[i] -= rm; negm[i] = -rm; }
                pend = false;
                SLOTM(sA, sB, pB0, pB1, pA0, pA1, 8192, 0, true, false, { if (r == 0) asm volatile("s_waitcnt vmcnt(0)" ::: "memory");
                    ABAR(16 * r + 1); VPRE(n0p, n1p, 0); });
                ADVANCE();
            }
#pragma unroll 1
            for (int t = 1; t < 16; ++t) {
                BASES();
                SLOTM(sB, sA, pA0, pA1, pB0, pB1, 0, 0, true, true, { VPRE(v0p, v1p, 8192); });
                SLOTM(sA, sB, pB0, pB1, pA0, pA1, 8192, 8192, true, true, { ABAR(16 * r + t + 1); VPRE(n0p, n1p, 0); });
                ADVANCE();
            }
            {
                BASES();
                SLOTM(sB, sA, pA0, pA1, pB0, pB1, 0, 0, false, true, { VPRE(v0p, v1p, 8192); });
                APPLY_PEND();
                o[0] = MFMA32(vf0, BC8(pB0), o[0]); o[1] = MFMA32(vf1, BC8(pB0), o[1]); vf1 = VRD(8192, 1, 1); vf0 = VRD(8192, 1, 0);
                o[2] = MFMA32(vf2, BC8(pB0), o[2]); o[3] = MFMA32(vf3, BC8(pB0), o[3]); vf3 = VRD(8192, 1, 3); vf2 = VRD(8192, 1, 2);
                o[0] = MFMA32(vf0, BC8(pB1), o[0]); o[1] = MFMA32(vf1, BC8(pB1), o[1]); o[2] = MFMA32(vf2, BC8(pB1), o[2]); o[3] = MFMA32(vf3, BC8(pB1), o[3]);
                if (r == 0) {
                    ETAG();
                    unsigned qa = lds0 + EX + 4096u * qb + lo16; asm volatile("" : "+v"(qa));
#pragma unroll
                    for (int s = 0; s < 8; ++s) qf[s] = ldsrd128((lds_cptr)(uintptr_t)qa + 16384 * (s >> 2) + 1024 * (s & 3));
                }
                const float inv = 1.0f / swap32sum(lsum[0] + lsum[1]);
                int ln = lane; asm volatile("" : "+v"(ln));
                const unsigned ovo = (unsigned)((ln >> 4) & 1) * 4096u + (unsigned)(ln & 15) * 16u + (unsigned)(ln >> 5) * 256u
                                   + (unsigned)((((g * 32 + h0 + r) * 8 + 2 * qb) * 4)) * 1024u;
                unsigned lo_ = lds0 + EX + 4096u * qb + (unsigned)((ln >> 4) & 1) * 16384u + (unsigned)(ln & 15) * 16u + (unsigned)(ln >> 5) * 256u; asm volatile("" : "+v"(lo_));
#pragma unroll
                for (int db = 0; db < 4; ++db)
#pragma unroll
                    for (int sg = 0; sg < 2; ++sg) { const bf16x8 pk_ = pack8s(o[db], sg, inv);
                        if (r == 0) *(LAS bf16x8*)(uintptr_t)(lo_ + 1024u * db + 512u * sg) = pk_; else { st16_sc1(BCU(pk_), ovo + 1024u * db + 512u * sg, srdO); pk2[2 * db + sg] = pk_; } }
#pragma unroll
                for (int i = 0; i < 4; ++i) o[i] = f32x16{};
                lsum = f32x2{0.f, 0.f};
            }
        }
                LBAR();
#pragma unroll
        for (int i = 0; i < 2; ++i)
#pragma unroll
            for (int c = 0; c < 9; ++c) oacc[i][c] = f32x4{};
    }
    {
        const int mg = w & 3, kq = w >> 2;
        int ln = lane; asm volatile("" : "+v"(ln));
        const unsigned ab0 = lds0 + (unsigned)(16 * ((ln >> 4) >> 1) + 4 * ((ln >> 4) & 1) + ((ln & 15) >> 2)) * 288u + 8u * (ln & 3);
#define TCHUNK(base, F) do { unsigned a0a = (base) + ab0; asm volatile("" : "+v"(a0a)); const lds_cptr kb_ = (lds_cptr)(uintptr_t)a0a; bf16x8 af_[18]; \
            _Pragma("unroll") for (int ks = 0; ks < 2; ++ks) _Pragma("unroll") for (int c = 0; c < 9; ++c) af_[9 * ks + c] = cat8(vtr(kb_ + 9216 * ks + 32 * c), vtr(kb_ + 9216 * ks + 2304 + 32 * c)); \
            SB(); \
            _Pragma("unroll") for (int ks = 0; ks < 2; ++ks) _Pragma("unroll") for (int c = 0; c < 9; ++c) { \
                oacc[0][c] = MFMA16(af_[9 * ks + c], (F)[2 * ks], oacc[0][c]); oacc[1][c] = MFMA16(af_[9 * ks + c], (F)[2 * ks + 1], oacc[1][c]); } \
            SB(); } while (0)
        if (kq == 0) {
            unsigned sw_ = lds0 + 73728u + 4096u * (unsigned)mg + (unsigned)((ln >> 4) & 1) * 2048u + (unsigned)(ln & 15) * 16u + (unsigned)(ln >> 5) * 256u;
            unsigned sr_ = lds0 + 73728u + 4096u * (unsigned)mg + (unsigned)ln * 16u;
            asm volatile("" : "+v"(sw_), "+v"(sr_));
#pragma unroll
            for (int hf = 0; hf < 2; ++hf) {
#pragma unroll
                for (int d = 0; d < 2; ++d)
#pragma unroll
                    for (int sg = 0; sg < 2; ++sg) *(LAS bf16x8*)(uintptr_t)(sw_ + 1024u * d + 512u * sg) = pk2[4 * hf + 2 * d + sg];
                asm volatile("" ::: "memory");
                bf16x8 f_[4];
#pragma unroll
                for (int d = 0; d < 2; ++d)
#pragma unroll
                    for (int m = 0; m < 2; ++m) f_[2 * d + m] = ldsrd128((lds_cptr)(uintptr_t)(sr_ + 2048u * m + 1024u * d));
                asm volatile("" ::: "memory");
                if (hf == 0) TCHUNK(WS1, f_); else TCHUNK(WS2, f_);
                if (hf == 0) {
                    asm volatile("s_waitcnt vmcnt(0)" ::: "memory");
                    if (ln == 0) st4_sc1(tag, WS_FO + (unsigned)((((g * 4 + hq) * 2 + 1) * 4 + mg) * 4 + c4) * 4u, srdF);
                }
            }
        }
        wait_tag(WS_FO + (unsigned)((((g * 4 + hq) * 2 + 1) * 4 + mg) * 4 + ((c4 + 1 + kq + (lane & 1)) & 3)) * 4u, srdF, tag);
                bf16x8 of[12];
#pragma unroll
        for (int i = 0; i < 6; ++i) {
            const int j2 = 4 + 6 * kq + i;
            const int hd = 8 * hq + 2 * ((c4 + (j2 >> 2)) & 3) + 1;
            const unsigned so = (unsigned)((((g * 32 + hd) * 8 + 2 * mg) * 4) + (j2 & 3)) * 1024u;
            LD16_SC1(of[2 * i], lo16, srdO, RFL(so)); LD16_SC1(of[2 * i + 1], lo16, srdO, RFL(so + 4096u));
        }
        asm volatile("s_waitcnt vmcnt(0)\n\ts_barrier" ::: "memory"); SB();
        asm volatile("" : "+v"(of[0]), "+v"(of[1]), "+v"(of[2]), "+v"(of[3]), "+v"(of[4]), "+v"(of[5]), "+v"(of[6]), "+v"(of[7]) :: "memory");
        asm volatile("" : "+v"(of[8]), "+v"(of[9]), "+v"(of[10]), "+v"(of[11]) :: "memory");
                TCHUNK(kq ? WCH : WS0, of); TCHUNK(kq ? 2u * WCH : WS3, of + 4); TCHUNK(kq ? 3u * WCH : 0u, of + 8);
        LBAR();
        const unsigned xw = (unsigned)w * 9216u + lo16, xr = (unsigned)(w ^ 4) * 9216u + lo16;
#pragma unroll
        for (int c = 0; c < 9; ++c) *(LAS f32x4*)(LW + xw + 1024 * c) = kq ? oacc[0][c] : oacc[1][c];
        LBAR();
        f32x4 fin[9];
#pragma unroll
        for (int c = 0; c < 9; ++c) { const f32x4 t4 = *(const LAS f32x4*)(LW + xr + 1024 * c); fin[c] = (kq ? oacc[1][c] : oacc[0][c]) + t4; }
        const int mb = 2 * mg + kq;
        const unsigned lo8 = (unsigned)lane * 8u;
                if (mg != hq) {
            const unsigned sso = (unsigned)((((g * 4 + c4) * 8 + mb) * 4 + hq) * 9) * 512u + lo8;
#pragma unroll
            for (int c = 0; c < 9; ++c) { const u32x2 v2 = {cvtpk(fin[c][0], fin[c][1]), cvtpk(fin[c][2], fin[c][3])}; st8_sc1(v2, sso + 512u * c, srdS); }
            asm volatile("s_waitcnt vmcnt(0)" ::: "memory");
            if (lane == 0) st4_sc1(tag, WS_FS + (unsigned)(((g * 4 + c4) * 8 + mb) * 4 + hq) * 4u, srdF);
        } else {
            const int ot = (hq + 1 + (lane % 3)) & 3;
            wait_tag(WS_FS + (unsigned)(((g * 4 + c4) * 8 + mb) * 4 + ot) * 4u, srdF, tag);
            if (hq == 0 && c4 == 0 && kq == 0 && lane == 0) st4_sc1(tag, WS_E + 64u * (unsigned)g, srdF);
            u32x2 sv[27];
#pragma unroll
            for (int m = 0; m < 3; ++m)
#pragma unroll
                for (int c = 0; c < 9; ++c) LD8_SC1(sv[9 * m + c], lo8, srdS, RFL((unsigned)((((g * 4 + c4) * 8 + mb) * 4 + ((hq + 1 + m) & 3)) * 9 + c) * 512u));
            asm volatile("s_waitcnt vmcnt(0)" : "+v"(sv[0]), "+v"(sv[1]), "+v"(sv[2]), "+v"(sv[3]), "+v"(sv[4]), "+v"(sv[5]), "+v"(sv[6]), "+v"(sv[7]), "+v"(sv[8]) :: "memory");
            asm volatile("" : "+v"(sv[9]), "+v"(sv[10]), "+v"(sv[11]), "+v"(sv[12]), "+v"(sv[13]), "+v"(sv[14]), "+v"(sv[15]), "+v"(sv[16]), "+v"(sv[17]) :: "memory");
            asm volatile("" : "+v"(sv[18]), "+v"(sv[19]), "+v"(sv[20]), "+v"(sv[21]), "+v"(sv[22]), "+v"(sv[23]), "+v"(sv[24]), "+v"(sv[25]), "+v"(sv[26]) :: "memory");
#pragma unroll
            for (int c = 0; c < 9; ++c) {
                f32x4 a = fin[c];
#pragma unroll
                for (int m = 0; m < 3; ++m) { const u32x2 v = sv[9 * m + c];
                    a[0] += __uint_as_float(v[0] << 16); a[1] += __uint_as_float(v[0] & 0xFFFF0000u); a[2] += __uint_as_float(v[1] << 16); a[3] += __uint_as_float(v[1] & 0xFFFF0000u); }
                const u32x2 ov = {cvtpk(a[0], a[1]), cvtpk(a[2], a[3])};
                *(u32x2*)(out + (size_t)(b * 128 + 16 * mb + (lane & 15)) * 1152 + 576 * p + 144 * c4 + 16 * c + 4 * g4) = ov;
            }
        }
    }
    }

extern "C" void kernel_launch(void* const* d_in, const int* in_sizes, int n_in, void* d_out, int out_size, void* d_ws, size_t ws_size, hipStream_t stream) {
    (void)hipFuncSetAttribute((const void*)fused_kernel, hipFuncAttributeMaxDynamicSharedMemorySize, LDS_BYTES);
    hipLaunchKernelGGL(fused_kernel, dim3(256), dim3(512), LDS_BYTES, stream,
                       (const char*)d_in[0], (const char*)d_in[1], (const char*)d_in[2], (const char*)d_in[3], (const char*)d_in[4],
                       (const char*)d_in[5], (const char*)d_in[6], (const char*)d_in[7], (const char*)d_in[8], (const char*)d_in[9],
                       (char*)d_ws, (unsigned short*)d_out);
}
```

```cpp
#include <hip/hip_runtime.h>
#include <hip/hip_bf16.h>
#include <cstdint>

typedef short bf16x8 __attribute__((ext_vector_type(8)));
typedef short s16x4 __attribute__((ext_vector_type(4)));
typedef float f32x16 __attribute__((ext_vector_type(16)));
typedef float f32x4 __attribute__((ext_vector_type(4)));
typedef float f32x2 __attribute__((ext_vector_type(2)));
typedef __bf16 bf16x2_t __attribute__((ext_vector_type(2)));
typedef unsigned u32x4 __attribute__((ext_vector_type(4)));
typedef unsigned u32x2 __attribute__((ext_vector_type(2)));
#define LAS __attribute__((address_space(3)))
#define DI __device__ __forceinline__
typedef LAS const char* lds_cptr;
typedef LAS char* lds_ptr;

constexpr int KVBYTES = 8 * 32 * 1024 * 256;
constexpr unsigned SLOT = 16384;
constexpr int LDS_BYTES = 163840;
constexpr float CSC = 0.08838834764831845f * 1.4426950408889634f;
constexpr float THR = 8.0f;
constexpr unsigned EX = 6 * SLOT, WR = 8 * SLOT;
constexpr unsigned WCH = 18432u, WS0 = 108544u, WS1 = WS0 + WCH, WS2 = WS0 + 2 * WCH, WS3 = WS0 - WCH;
constexpr unsigned WS_E = 0, WS_FO = 4096, WS_FS = 16384;
constexpr size_t WS_O = 65536, WS_O_BYTES = (size_t)2048 * 4096 * 2;
constexpr size_t WS_SL = WS_O + WS_O_BYTES, WS_SL_BYTES = (size_t)16 * 4 * 8 * 4 * 9 * 512;
constexpr unsigned SPIN_CAP = 1u << 20;

#define MFMA32(a, b, c) __builtin_amdgcn_mfma_f32_32x32x16_bf16((a), (b), (c), 0, 0, 0)
#define MFMA16(a, b, c) __builtin_amdgcn_mfma_f32_16x16x32_bf16((a), (b), (c), 0, 0, 0)
#define RFL(x) __builtin_amdgcn_readfirstlane(x)
#define WAITBAR(N) asm volatile("s_waitcnt vmcnt(" #N ") lgkmcnt(0)\n\ts_barrier" ::: "memory")
template <int N> __device__ __forceinline__ void waitbar() { asm volatile("s_waitcnt vmcnt(%0) lgkmcnt(0)\n\ts_barrier" :: "i"(N) : "memory"); }
template <int N> __device__ __forceinline__ void waitv() { asm volatile("s_waitcnt vmcnt(%0)" :: "i"(N) : "memory"); }
#define WB17 WAITBAR(17)
#define WWIN 3
#define XWIN 4
#define LBAR() asm volatile("s_waitcnt lgkmcnt(0)\n\ts_barrier" ::: "memory")
#define SB() __builtin_amdgcn_sched_barrier(0)
#define PIN(x) asm volatile("" : "+v"(x))
#define PIN2(x) asm volatile("" : "+v"(x))

DI unsigned cvtpk(float lo, float hi) { f32x2 v = {lo, hi}; bf16x2_t b = __builtin_convertvector(v, bf16x2_t); return __builtin_bit_cast(unsigned, b); }
DI s16x4 vtr(lds_cptr p) { return __builtin_bit_cast(s16x4, __builtin_amdgcn_ds_read_tr16_b64_v4i16((LAS s16x4*)p)); }
DI bf16x8 cat8(s16x4 a, s16x4 b) { return __builtin_shufflevector(a, b, 0, 1, 2, 3, 4, 5, 6, 7); }
DI bf16x8 ldsrd128(lds_cptr p) { return *(const LAS bf16x8*)p; }
DI float swap32max(float m) { auto rr = __builtin_amdgcn_permlane32_swap(__float_as_uint(m), __float_as_uint(m), false, false); return __builtin_fmaxf(__uint_as_float(rr[0]), __uint_as_float(rr[1])); }
DI float swap32sum(float m) { auto rr = __builtin_amdgcn_permlane32_swap(__float_as_uint(m), __float_as_uint(m), false, false); return __uint_as_float(rr[0]) + __uint_as_float(rr[1]); }
DI bf16x8 pack8s(const f32x16& a, int sg, float sc) {
    u32x4 wv = {cvtpk(a[8 * sg + 0] * sc, a[8 * sg + 1] * sc), cvtpk(a[8 * sg + 2] * sc, a[8 * sg + 3] * sc), cvtpk(a[8 * sg + 4] * sc, a[8 * sg + 5] * sc), cvtpk(a[8 * sg + 6] * sc, a[8 * sg + 7] * sc)};
    return __builtin_bit_cast(bf16x8, wv);
}
#define BC8(x) __builtin_bit_cast(bf16x8, x)
#define BCU(x) __builtin_bit_cast(u32x4, x)

DI void dma16_nt(unsigned ldsdst, unsigned voff, __amdgpu_buffer_rsrc_t srd, unsigned soff) {
    asm volatile("s_mov_b32 m0, %0\n\ts_nop 0\n\tbuffer_load_dwordx4 %1, %2, %3 offen nt lds" :: "s"(ldsdst), "v"(voff), "s"(srd), "s"(soff) : "memory");
}
DI void dma16(unsigned ldsdst, unsigned voff, __amdgpu_buffer_rsrc_t srd, unsigned soff) {
    asm volatile("s_mov_b32 m0, %0\n\ts_nop 0\n\tbuffer_load_dwordx4 %1, %2, %3 offen lds" :: "s"(ldsdst), "v"(voff), "s"(srd), "s"(soff) : "memory");
}
DI void st16_sc1(u32x4 v, unsigned voff, __amdgpu_buffer_rsrc_t srd) { __builtin_amdgcn_raw_buffer_store_b128(v, srd, (int)voff, 0, 16); }
DI void st8_sc1(u32x2 v, unsigned voff, __amdgpu_buffer_rsrc_t srd) { __builtin_amdgcn_raw_buffer_store_b64(v, srd, (int)voff, 0, 16); }
DI void st4_sc1(unsigned v, unsigned voff, __amdgpu_buffer_rsrc_t srd) { __builtin_amdgcn_raw_buffer_store_b32(v, srd, (int)voff, 0, 16); }
#define LD16_SC1(dst, voff, srd, soff) asm volatile("buffer_load_dwordx4 %0, %1, %2, %3 offen sc1" : "=v"(dst) : "v"(voff), "s"(srd), "s"(soff) : "memory")
#define LD8_SC1(dst, voff, srd, soff) asm volatile("buffer_load_dwordx2 %0, %1, %2, %3 offen sc1" : "=v"(dst) : "v"(voff), "s"(srd), "s"(soff) : "memory")
#define LD4_SC1(dst, voff, srd) asm volatile("buffer_load_dword %0, %1, %2, 0 offen sc1" : "=v"(dst) : "v"(voff), "s"(srd) : "memory")
#define LD16(dst, voff, srd, soff) asm volatile("buffer_load_dwordx4 %0, %1, %2, %3 offen" : "=v"(dst) : "v"(voff), "s"(srd), "s"(soff) : "memory")
DI void wait_tag(unsigned voff, __amdgpu_buffer_rsrc_t srd, unsigned tag) {
    for (unsigned sp = 0; sp < SPIN_CAP; ++sp) {
        unsigned f;
        asm volatile("buffer_load_dword %0, %1, %2, 0 offen sc1\n\ts_waitcnt vmcnt(0)" : "=v"(f) : "v"(voff), "s"(srd) : "memory");
        if (__all(f == tag)) break;
        __builtin_amdgcn_s_sleep(1);
    }
}

__global__ __launch_bounds__(512, 2) void fused_kernel(const char* __restrict__ xA, const char* __restrict__ kA, const char* __restrict__ vA, const char* __restrict__ wqA, const char* __restrict__ woA,
                                                      const char* __restrict__ xB, const char* __restrict__ kB, const char* __restrict__ vB, const char* __restrict__ wqB, const char* __restrict__ woB,
                                                      char* __restrict__ ws, unsigned short* __restrict__ out) {
    extern __shared__ __attribute__((aligned(16))) char lds[];
    const int tid = threadIdx.x, lane = tid & 63;
    const int w = RFL(tid >> 6);
    const int r32 = lane & 31, hi = lane >> 5;
    const int wg = blockIdx.x, xcd = wg & 7, idx = wg >> 3;
    const int p = xcd & 1, hq = xcd >> 1, c4 = idx & 3, b = idx >> 2;
    const int g = p * 8 + b;
    const int h0 = 8 * hq + 2 * c4;
    const unsigned lds0 = (unsigned)(uintptr_t)lds;
    const lds_cptr L = (lds_cptr)lds;
    const lds_ptr LW = (lds_ptr)lds;
        const __amdgpu_buffer_rsrc_t srdX = __builtin_amdgcn_make_buffer_rsrc((void*)(p ? xB : xA), (short)0, 8 * 128 * 576 * 2, 0x00020000);
    const __amdgpu_buffer_rsrc_t srdW = __builtin_amdgcn_make_buffer_rsrc((void*)(p ? wqB : wqA), (short)0, 576 * 4096 * 2, 0x00020000);
    const __amdgpu_buffer_rsrc_t srdK = __builtin_amdgcn_make_buffer_rsrc((void*)(p ? kB : kA), (short)0, KVBYTES, 0x00020000);
    const __amdgpu_buffer_rsrc_t srdV = __builtin_amdgcn_make_buffer_rsrc((void*)(p ? vB : vA), (short)0, KVBYTES, 0x00020000);
    const __amdgpu_buffer_rsrc_t srdWo = __builtin_amdgcn_make_buffer_rsrc((void*)(p ? woB : woA), (short)0, 4096 * 576 * 2, 0x00020000);
    const __amdgpu_buffer_rsrc_t srdO = __builtin_amdgcn_make_buffer_rsrc((void*)(ws + WS_O), (short)0, (int)WS_O_BYTES, 0x00020000);
    const __amdgpu_buffer_rsrc_t srdS = __builtin_amdgcn_make_buffer_rsrc((void*)(ws + WS_SL), (short)0, (int)WS_SL_BYTES, 0x00020000);
    const __amdgpu_buffer_rsrc_t srdF = __builtin_amdgcn_make_buffer_rsrc((void*)ws, (short)0, 65536, 0x00020000);

    const bool isB = w >= 4;
    const int bw = w & 3;
    const int r7 = (lane >> 2) & 7;
    const int blk = (lane >> 4) & 1, q4 = (lane & 15) >> 2, p4 = lane & 3, g4 = lane >> 4;
    const unsigned lo16 = (unsigned)lane * 16u;
    unsigned ev;
#define ELOAD() do { const unsigned eo_ = WS_E + 64u * (unsigned)g; asm volatile("buffer_load_dword %0, %1, %2, 0 offen sc1" : "=v"(ev) : "v"(eo_), "s"(srdF) : "memory"); } while (0)
#define ETAG() do { PIN(ev); tag = (unsigned)RFL(ev) * 0x9E3779B1u + 0x7F4A7C15u; } while (0)
    const unsigned chs0 = 16u * (4u * (unsigned)hi + (unsigned)((lane & 3) ^ ((2 * (2 * bw) + (r7 >> 2)) & 3)));
    const unsigned chs1 = 16u * (4u * (unsigned)hi + (unsigned)((lane & 3) ^ ((2 * (2 * bw + 1) + (r7 >> 2)) & 3)));
    const unsigned srcKV0 = (unsigned)(16 * bw + r7) * 256u + chs0, srcKV1 = (unsigned)(16 * bw + 8 + r7) * 256u + chs1;
    const unsigned srcW0 = (unsigned)(16 * bw + r7) * 8192u + chs0, srcW1 = (unsigned)(16 * bw + 8 + r7) * 8192u + chs1;
    auto kv_off = [&](int T) -> unsigned { return (unsigned)((b * 32 + h0) * 1024 + 64 * T) * 256u; };
    auto kslot = [&](int T) -> unsigned { return (unsigned)(T % 3) * SLOT; };
    auto vslot = [&](int T) -> unsigned { return (unsigned)(3 + T % 3) * SLOT; };
    auto dma_tile = [&](__amdgpu_buffer_rsrc_t srd, int T, unsigned slot) { const unsigned so = kv_off(T), d = lds0 + 4096u * (unsigned)bw + slot;
        dma16_nt(RFL(d), srcKV0, srd, RFL(so)); dma16_nt(RFL(d + 1024), srcKV0, srd, RFL(so + 128));
        dma16_nt(RFL(d + 2048), srcKV1, srd, RFL(so)); dma16_nt(RFL(d + 3072), srcKV1, srd, RFL(so + 128)); };
    auto dma_k = [&](int T) { dma_tile(srdK, T, kslot(T)); };
    auto dma_v = [&](int T) { dma_tile(srdV, T, vslot(T)); };
    const int rot = (b + 2 * c4) % 9;
    auto dma_wjob = [&](int head, int n, unsigned dst) {
        const int ps = n / 9, kc0 = n - 9 * ps + rot, kc = kc0 >= 9 ? kc0 - 9 : kc0;
        const unsigned sw = (unsigned)(64 * kc) * 8192u + (unsigned)head * 256u + 128u * (unsigned)ps, d = lds0 + dst + 2048u * (unsigned)bw;
        dma16(RFL(d), srcW0, srdW, RFL(sw)); dma16(RFL(d + 1024), srcW1, srdW, RFL(sw));
    };
    auto dma_wdj = [&](int head, int gj, unsigned dst) { dma_wjob(head, 2 * gj, dst); dma_wjob(head, 2 * gj + 1, dst + 8192u); };
    unsigned tag;

    bf16x8 pk2[8];
    f32x4 oacc[2][9];
    if (isB) {
        bf16x8 xf[36];
                const unsigned vx = (unsigned)(32 * bw + (lane >> 3)) * 1152u + 16u * (lane & 7);
        const unsigned sx = (unsigned)(b * 128) * 1152u;
#define XLOAD(c) do { const int kk_ = (c) + rot; const unsigned so_ = sx + 128u * (unsigned)(kk_ >= 9 ? kk_ - 9 : kk_); \
            LD16(xf[4 * (c)], vx, srdX, RFL(so_)); LD16(xf[4 * (c) + 1], vx, srdX, RFL(so_ + 9216u)); LD16(xf[4 * (c) + 2], vx, srdX, RFL(so_ + 18432u)); LD16(xf[4 * (c) + 3], vx, srdX, RFL(so_ + 27648u)); } while (0)
        XLOAD(0); XLOAD(1); XLOAD(2); XLOAD(3);
        if (XWIN > 4) XLOAD(4); if (XWIN > 5) XLOAD(5); if (XWIN > 6) XLOAD(6); if (XWIN > 7) XLOAD(7); if (XWIN > 8) XLOAD(8);
        const unsigned xwe = lds0 + EX + 4096u * bw + (unsigned)(lane >> 3) * 128u + 16u * (unsigned)((lane & 7) ^ ((lane >> 4) & 7));
        const unsigned xwo = lds0 + EX + 4096u * bw + (unsigned)(8 + (lane >> 3)) * 128u + 16u * (unsigned)((lane & 7) ^ ((4 + (lane >> 4)) & 7));
        const unsigned xrd = lds0 + EX + 4096u * bw + (unsigned)r32 * 128u + 16u * (unsigned)(hi ^ ((r32 >> 1) & 7));
        const unsigned wb0 = lds0 + 1024u * hi + 64u * (0 + q4) + 16u * ((2 * blk + (p4 & 1)) ^ (2 * hi + 0)) + 8u * (p4 >> 1);
        const unsigned wb1 = lds0 + 1024u * hi + 64u * (4 + q4) + 16u * ((2 * blk + (p4 & 1)) ^ (2 * hi + 1)) + 8u * (p4 >> 1);
        const unsigned qex = EX + 4096u * bw + lo16;
        unsigned tch = 0;
#define TOUCH(vo, srd, so) asm volatile("buffer_load_dword %0, %1, %2, %3 offen" : "+v"(tch) : "v"(vo), "s"(srd), "s"(so) : "memory")
#define TOUCH_WQ() do { int l_ = lane < 36 ? lane : 35; asm volatile("" : "+v"(l_)); const unsigned so_ = (unsigned)(72 * b + 18 * bw) * 8192u + (unsigned)(h0 + 1) * 256u; \
            TOUCH((unsigned)(l_ >> 1) * 8192u + 128u * (l_ & 1), srdW, RFL(so_)); } while (0)
#define TOUCH_WO(r) do { int l_ = lane < 36 ? lane : 35; asm volatile("" : "+v"(l_)); const unsigned so_ = (unsigned)(128 * (h0 + (r)) + 16 * b + 4 * bw) * 1152u; \
            TOUCH((unsigned)l_ * 128u, srdWo, RFL(so_)); } while (0)
        f32x16 acc[2];
        bf16x8 ar[8];
#define XCONV(c) do { asm volatile("s_waitcnt vmcnt(%4)" : "+v"(xf[4 * (c)]), "+v"(xf[4 * (c) + 1]), "+v"(xf[4 * (c) + 2]), "+v"(xf[4 * (c) + 3]) : "i"(4 * ((((c) - 1 + XWIN < 8) ? (c) - 1 + XWIN : 8) - (c)) + ((c) + XWIN > 8 ? 1 : 0)) : "memory"); \
            *(LAS bf16x8*)(uintptr_t)(xwe) = xf[4 * (c)]; *(LAS bf16x8*)(uintptr_t)(xwo) = xf[4 * (c) + 1]; *(LAS bf16x8*)(uintptr_t)(xwe + 2048) = xf[4 * (c) + 2]; *(LAS bf16x8*)(uintptr_t)(xwo + 2048) = xf[4 * (c) + 3]; \
            asm volatile("" ::: "memory"); \
            _Pragma("unroll") for (int ks = 0; ks < 4; ++ks) xf[4 * (c) + ks] = ldsrd128((lds_cptr)(uintptr_t)(xrd ^ (32u * ks))); \
            asm volatile("" ::: "memory"); \
            if ((c) + XWIN <= 8) XLOAD(((c) + XWIN <= 8) ? (c) + XWIN : 0); if ((c) + XWIN == 8) TOUCH_WQ(); } while (0)
#define WFR(wp0, wp1, m) cat8(vtr((wp0) + 8192 * ((m) >> 3) + 2048 * (((m) >> 1) & 3) + 512 * ((m) & 1)), vtr((wp1) + 8192 * ((m) >> 3) + 2048 * (((m) >> 1) & 3) + 512 * ((m) & 1)))
#define WPTR(p0, p1, base) unsigned p0##a = wb0 + (base), p1##a = wb1 + (base); asm volatile("" : "+v"(p0##a), "+v"(p1##a)); const lds_cptr p0 = (lds_cptr)(uintptr_t)p0##a, p1 = (lds_cptr)(uintptr_t)p1##a
#define QMF(gj, m) do { constexpr int n_ = 2 * (gj) + ((m) >> 3), kc_ = n_ % 9, ks_ = ((m) >> 1) & 3, dl_ = (m) & 1; \
            if (kc_ == 0 && ks_ == 0) acc[dl_] = MFMA32(ar[(m) & 7], xf[4 * kc_ + ks_], (f32x16{})); else acc[dl_] = MFMA32(ar[(m) & 7], xf[4 * kc_ + ks_], acc[dl_]); \
            PIN(acc[dl_]); \
            if (kc_ == 8 && ((m) & 7) == 7) { _Pragma("unroll") for (int dl = 0; dl < 2; ++dl) _Pragma("unroll") for (int sg = 0; sg < 2; ++sg) \
                *(LAS bf16x8*)(LW + qex + 16384u * (n_ / 9) + (2 * dl + sg) * 1024) = pack8s(acc[dl], sg, CSC); } } while (0)
#define QSTREAM(gj, base, MID, HASNEXT, nbase) do { \
            { WPTR(c0, c1, base); \
              QMF(gj, 0); ar[6] = WFR(c0, c1, 6); SB(); QMF(gj, 1); ar[7] = WFR(c0, c1, 7); SB(); QMF(gj, 2); ar[0] = WFR(c0, c1, 8); SB(); QMF(gj, 3); ar[1] = WFR(c0, c1, 9); SB(); \
              QMF(gj, 4); ar[2] = WFR(c0, c1, 10); SB(); QMF(gj, 5); ar[3] = WFR(c0, c1, 11); SB(); QMF(gj, 6); ar[4] = WFR(c0, c1, 12); SB(); QMF(gj, 7); ar[5] = WFR(c0, c1, 13); SB(); \
              QMF(gj, 8); ar[6] = WFR(c0, c1, 14); SB(); QMF(gj, 9); ar[7] = WFR(c0, c1, 15); SB(); QMF(gj, 10); SB(); QMF(gj, 11); SB(); } \
            MID; SB(); \
            if (HASNEXT) { WPTR(n0, n1, nbase); \
              QMF(gj, 12); ar[0] = WFR(n0, n1, 0); ar[1] = WFR(n0, n1, 1); SB(); QMF(gj, 13); ar[2] = WFR(n0, n1, 2); ar[3] = WFR(n0, n1, 3); SB(); \
              QMF(gj, 14); SB(); QMF(gj, 15); ar[4] = WFR(n0, n1, 4); ar[5] = WFR(n0, n1, 5); SB(); } \
            else { QMF(gj, 12); SB(); QMF(gj, 13); SB(); QMF(gj, 14); SB(); QMF(gj, 15); SB(); } \
        } while (0)
#define QDJ(T, gj, base) do { WPTR(c0, c1, base); \
            dma_k((T) + 2); \
            _Pragma("unroll") for (int m = 0; m < 8; ++m) ar[m] = WFR(c0, c1, m); SB(); \
            QMF(gj, 0); QMF(gj, 1); QMF(gj, 2); QMF(gj, 3); QMF(gj, 4); QMF(gj, 5); QMF(gj, 6); QMF(gj, 7); SB(); \
            dma_v((T) + 1); \
            _Pragma("unroll") for (int m = 8; m < 16; ++m) ar[m & 7] = WFR(c0, c1, m); SB(); \
            QMF(gj, 8); QMF(gj, 9); QMF(gj, 10); QMF(gj, 11); QMF(gj, 12); QMF(gj, 13); QMF(gj, 14); QMF(gj, 15); SB(); } while (0)
                XCONV(0); XCONV(1);
        LBAR();         { WPTR(c0, c1, 0u); ar[0] = WFR(c0, c1, 0); ar[1] = WFR(c0, c1, 1); ar[2] = WFR(c0, c1, 2); ar[3] = WFR(c0, c1, 3); ar[4] = WFR(c0, c1, 4); ar[5] = WFR(c0, c1, 5); }
        QSTREAM(0, 0u, LBAR(), true, SLOT);     XCONV(2); XCONV(3);         QSTREAM(1, SLOT, LBAR(), true, 3 * SLOT);          XCONV(4); XCONV(5);
        QSTREAM(2, 3 * SLOT, LBAR(), true, 2 * SLOT);      XCONV(6); XCONV(7);
        QSTREAM(3, 2 * SLOT, LBAR(), true, 4 * SLOT);      XCONV(8);
        QSTREAM(4, 4 * SLOT, LBAR(), true, 5 * SLOT);         QSTREAM(5, 5 * SLOT, LBAR(), true, EX + SLOT);
        QSTREAM(6, EX + SLOT, LBAR(), true, WR);
        QSTREAM(7, WR, LBAR(), true, WR + SLOT);
        QSTREAM(8, WR + SLOT, {}, false, 0u);
                LBAR();
#define BKV(T) do { if ((T) < 30) dma_k((T) + 2); if ((T) < 31) dma_v((T) + 1); } while (0)
        BKV(0); dma_wdj(h0 + 1, 0, WR); dma_wdj(h0 + 1, 1, WR + SLOT); WAITBAR(16);
        BKV(1); ELOAD(); WAITBAR(9);
        QDJ(2, 0, WR); WAITBAR(8);
        QDJ(3, 1, WR + SLOT); ETAG(); dma_wdj(h0 + 1, 2, WR); WAITBAR(12);
        BKV(4); dma_wdj(h0 + 1, 3, WR + SLOT); WAITBAR(12);
        QDJ(5, 2, WR); WAITBAR(8);
        QDJ(6, 3, WR + SLOT); dma_wdj(h0 + 1, 4, WR); WAITBAR(12);
        BKV(7); dma_wdj(h0 + 1, 5, WR + SLOT); WAITBAR(12);
        QDJ(8, 4, WR); WAITBAR(8);
        QDJ(9, 5, WR + SLOT); dma_wdj(h0 + 1, 6, WR); WAITBAR(12);
        BKV(10); dma_wdj(h0 + 1, 7, WR + SLOT); WAITBAR(12);
        QDJ(11, 6, WR); WAITBAR(8);
        QDJ(12, 7, WR + SLOT); dma_wdj(h0 + 1, 8, WR); WAITBAR(12);
        BKV(13); TOUCH_WO(0); TOUCH_WO(1);
        {
            const char* a_ = (lane & 1) ? (const char*)out + (size_t)(b * 128 + 16 * (2 * hq + (bw & 1))) * 2304 + 1152 * p + 288 * c4 : ws + WS_SL + (size_t)(g * 4 + c4) * 147456 + 36864 * bw;
            asm volatile("global_load_dword %0, %1, off sc1" : "+v"(tch) : "v"(a_) : "memory"); }
        WAITBAR(11);
        QDJ(14, 8, WR); WAITBAR(8);
        BKV(15); WAITBAR(8);
        {
            int ln = lane; asm volatile("" : "+v"(ln));
            unsigned wov[5];
#pragma unroll
            for (int i = 0; i < 5; ++i) {
                const int pc = (i < 4) ? bw + 4 * i : 16 + (bw & 1);
                const int uu = 64 * pc + ln, pos = uu / 18, ch = uu - 18 * pos;
                const int k = (pos & ~12) | ((pos & 4) << 1) | ((pos & 8) >> 1);
                wov[i] = (unsigned)k * 1152u + 16u * ch;
            }
            const unsigned ab0 = lds0 + (unsigned)(16 * ((ln >> 4) >> 1) + 4 * ((ln >> 4) & 1) + ((ln & 15) >> 2)) * 288u + 8u * (ln & 3);
            auto dma_wo = [&](int r, int jj, unsigned dst) {
                const int hd = 8 * hq + 2 * ((c4 + (jj >> 1)) & 3) + r;
                const unsigned so = (unsigned)(128 * hd + 64 * (jj & 1)) * 1152u + 288u * c4;
#pragma unroll
                for (int i = 0; i < 5; ++i) { const int pc = (i < 4) ? bw + 4 * i : 16 + (bw & 1); dma16(RFL(lds0 + dst + 1024u * pc), wov[i], srdWo, RFL(so)); }
            };
#pragma unroll
            for (int i = 0; i < 2; ++i)
#pragma unroll
                for (int c = 0; c < 9; ++c) oacc[i][c] = f32x4{};
            bf16x8 ofx[4], ofy[4];
            unsigned flagv;
            const unsigned fo1 = WS_FO + (unsigned)((((g * 4 + hq) * 2 + 0) * 4 + bw) * 4 + (ln & 3)) * 4u;
#define O1L(OF, jj) do { const int hd_ = 8 * hq + 2 * ((c4 + ((jj) >> 1)) & 3); const unsigned so_ = (unsigned)((((g * 32 + hd_) * 8 + 2 * bw) * 4) + 2 * ((jj) & 1)) * 1024u; \
                LD16_SC1(OF[0], lo16, srdO, RFL(so_)); LD16_SC1(OF[1], lo16, srdO, RFL(so_ + 4096u)); LD16_SC1(OF[2], lo16, srdO, RFL(so_ + 1024u)); LD16_SC1(OF[3], lo16, srdO, RFL(so_ + 5120u)); } while (0)
#define OPHALF(base, OF, ks) do { \
                unsigned a0a = ab0 + (base) + 9216u * (ks); asm volatile("" : "+v"(a0a)); \
                const lds_cptr kb_ = (lds_cptr)(uintptr_t)a0a; bf16x8 af_[9]; \
                _Pragma("unroll") for (int c = 0; c < 9; ++c) af_[c] = cat8(vtr(kb_ + 32 * c), vtr(kb_ + 2304 + 32 * c)); \
                SB(); \
                _Pragma("unroll") for (int c = 0; c < 9; ++c) { oacc[0][c] = MFMA16(af_[c], OF[2 * (ks)], oacc[0][c]); oacc[1][c] = MFMA16(af_[c], OF[2 * (ks) + 1], oacc[1][c]); PIN(oacc[0][c]); PIN(oacc[1][c]); } \
                SB(); \
            } while (0)
#define WSL(jj) ((jj) % 3 == 0 ? WS2 : (jj) % 3 == 1 ? WS0 : WS1)
#define ISSUE1(jj, OF) do { dma_wo(0, jj, WSL(jj)); O1L(OF, jj); } while (0)
#define BKVC(T, jj, OF) do { asm volatile("" : "+v"(OF[0]), "+v"(OF[1]), "+v"(OF[2]), "+v"(OF[3]) :: "memory"); \
                if ((T) < 30) dma_k((T) + 2); OPHALF(WSL(jj), OF, 0); if ((T) < 31) dma_v((T) + 1); OPHALF(WSL(jj), OF, 1); } while (0)
            BKV(16); WAITBAR(8);
            {
                unsigned oa = lds0 + EX + 4096u * bw + lo16; asm volatile("" : "+v"(oa)); const lds_cptr op = (lds_cptr)(uintptr_t)oa;
                ofx[0] = ldsrd128(op); ofx[1] = ldsrd128(op + 16384); ofx[2] = ldsrd128(op + 1024); ofx[3] = ldsrd128(op + 17408);
                ofy[0] = ldsrd128(op + 2048); ofy[1] = ldsrd128(op + 18432); ofy[2] = ldsrd128(op + 3072); ofy[3] = ldsrd128(op + 19456); }
            BKV(17); dma_wo(0, 0, WSL(0));
            {   const unsigned so_ = (unsigned)((((g * 32 + h0) * 8 + 2 * bw) * 4)) * 1024u + lo16;
#pragma unroll
                for (int i = 0; i < 4; ++i) { st16_sc1(BCU(ofx[i]), so_ + 4096u * (i & 1) + 1024u * (i >> 1), srdO); st16_sc1(BCU(ofy[i]), so_ + 4096u * (i & 1) + 1024u * (2 + (i >> 1)), srdO); } }
            WAITBAR(21);
            BKV(18); dma_wo(0, 1, WSL(1)); WAITBAR(13);
            if (ln == 0) st4_sc1(tag, WS_FO + (unsigned)((((g * 4 + hq) * 2 + 0) * 4 + bw) * 4 + c4) * 4u, srdF);
            BKVC(19, 0, ofx); WAITBAR(8);
            BKVC(20, 1, ofy); WAITBAR(8);
            LD4_SC1(flagv, fo1, srdF);
            BKV(21); WAITBAR(8);
            BKV(22);
            asm volatile("" : "+v"(flagv) :: "memory");
            if (!__all(flagv == tag)) wait_tag(fo1, srdF, tag);
            ISSUE1(2, ofx); WB17; asm volatile("" :: "v"(tch));
            BKV(23); ISSUE1(3, ofy); WB17;
            BKVC(24, 2, ofx); ISSUE1(4, ofx); WB17;
            BKVC(25, 3, ofy); ISSUE1(5, ofy); WB17;
            BKVC(26, 4, ofx); ISSUE1(6, ofx); WB17;
            BKVC(27, 5, ofy); ISSUE1(7, ofy); WB17;
            BKVC(28, 6, ofx); dma_wo(1, 0, WS1); WAITBAR(13);
            BKVC(29, 7, ofy); dma_wo(1, 1, WS2); WAITBAR(13);
            BKV(30); dma_wo(1, 2, WS0); WAITBAR(9);
            BKV(31); dma_wo(1, 3, WS3); WAITBAR(5);
                                    WAITBAR(0);
            dma_wo(1, 4, 0u); dma_wo(1, 5, WCH); dma_wo(1, 6, 2u * WCH);
            dma_wo(1, 7, 3u * WCH);
        }
    } else {
        {
            constexpr unsigned PS[9] = {0u, SLOT, 3 * SLOT, 2 * SLOT, 4 * SLOT, 5 * SLOT, EX + SLOT, WR, WR + SLOT};
#pragma unroll
            for (int s = 0; s < WWIN; ++s) dma_wdj(h0, s, PS[s]);
            #define PSTEP(s) do { waitbar<4 * (((s) - 1 + WWIN < 8 ? (s) - 1 + WWIN : 8) - (s)) + ((s) == 7 ? 4 : (s) == 8 ? 12 : 0)>(); \
                if ((s) + WWIN <= 8) dma_wdj(h0, ((s) + WWIN <= 8) ? (s) + WWIN : 0, PS[((s) + WWIN <= 8) ? (s) + WWIN : 0]); } while (0)
            PSTEP(0); PSTEP(1); PSTEP(2); PSTEP(3); PSTEP(4); PSTEP(5);
            PSTEP(6); dma_k(0);
            PSTEP(7); dma_k(1); dma_v(0);
            PSTEP(8);
            WAITBAR(8);
            ELOAD();
        }
        const int qb = w;
        bf16x8 qf[8];
        { unsigned qa = lds0 + EX + 4096u * qb + lo16; asm volatile("" : "+v"(qa));
#pragma unroll
          for (int s = 0; s < 8; ++s) qf[s] = ldsrd128((lds_cptr)(uintptr_t)qa + 16384 * (s >> 2) + 1024 * (s & 3)); }
        const unsigned kb0 = 2048u * (r32 >> 3) + 64u * (r32 & 7) + 16u * ((0 + hi) ^ ((r32 >> 2) & 3));
        const unsigned kb1 = 2048u * (r32 >> 3) + 64u * (r32 & 7) + 16u * ((2 + hi) ^ ((r32 >> 2) & 3));
        const unsigned vb0 = 64u * (4 * hi + q4) + 16u * ((2 * blk + (p4 & 1)) ^ (0 + hi)) + 8u * (p4 >> 1);
        const unsigned vb1 = 2048u + 64u * (4 * hi + q4) + 16u * ((2 * blk + (p4 & 1)) ^ (2 + hi)) + 8u * (p4 >> 1);
        f32x16 o[4];
#pragma unroll
        for (int i = 0; i < 4; ++i) o[i] = f32x16{};
        f32x2 lsum = {0.f, 0.f};
        float fpend = 1.f; bool pend = false;
        f32x16 sA = f32x16{}, sB = f32x16{}, negm = f32x16{};
        u32x4 pA0 = {}, pA1 = {}, pB0 = {}, pB1 = {};
        bf16x8 vf0 = {}, vf1 = {}, vf2 = {}, vf3 = {};
        const unsigned kbb0 = lds0 + kb0, kbb1 = lds0 + kb1, vbb0 = lds0 + vb0 + 3 * SLOT, vbb1 = lds0 + vb1 + 3 * SLOT;
        unsigned scur = 0, sprev = 2 * SLOT;
#define KRD(kp, ks) ldsrd128((((ks) & 1) ? k1p : k0p) + (kp) + 512 * ((ks) >> 1))
#define VRDX(a0, a1, vp, sg, db) cat8(vtr((a0) + (vp) + 4096 * (sg) + 512 * (db)), vtr((a1) + (vp) + 4096 * (sg) + 512 * (db)))
#define VRD(vp, sg, db) VRDX(v0p, v1p, vp, sg, db)
#define BASES() asm volatile("" : "+s"(scur), "+s"(sprev)); \
            unsigned k0a = kbb0 + scur, k1a = kbb1 + scur, v0a = vbb0 + sprev, v1a = vbb1 + sprev, n0a = vbb0 + scur, n1a = vbb1 + scur; \
            asm volatile("" : "+v"(k0a), "+v"(k1a), "+v"(v0a), "+v"(v1a), "+v"(n0a), "+v"(n1a)); \
            const lds_cptr k0p = (lds_cptr)(uintptr_t)k0a, k1p = (lds_cptr)(uintptr_t)k1a, v0p = (lds_cptr)(uintptr_t)v0a, v1p = (lds_cptr)(uintptr_t)v1a, n0p = (lds_cptr)(uintptr_t)n0a, n1p = (lds_cptr)(uintptr_t)n1a
#define ADVANCE() do { sprev = scur; scur = (scur == 2 * SLOT) ? 0u : scur + SLOT; } while (0)
#define EX1(S, i) do { S[i] = __builtin_amdgcn_exp2f(S[i]); } while (0)
#define MX3(a, b, c) __builtin_fmaxf(__builtin_fmaxf(a, b), c)
#define AMAX3(d, a, b, c) asm volatile("v_max3_f32 %0, %1, %2, %3" : "=v"(d) : "v"(a), "v"(b), "v"(c))
#define AMAX2(d, a, b) asm volatile("v_max_f32 %0, %1, %2" : "=v"(d) : "v"(a), "v"(b))
#define PAIR(S, i) (f32x2{S[2 * (i)], S[2 * (i) + 1]})
#define RMAX1(S) ma_ = MX3(S[0], S[1], S[2]); mb_ = MX3(S[3], S[4], S[5]); mc_ = MX3(S[6], S[7], S[8]); md_ = MX3(S[9], S[10], S[11]); me_ = MX3(S[12], S[13], S[14]); \
            ma_ = MX3(ma_, mb_, mc_); md_ = MX3(md_, me_, S[15]); ma_ = MX3(ma_, md_, 0.f); PIN(ma_)
#define RMAX2() do { auto rr_ = __builtin_amdgcn_permlane32_swap(__float_as_uint(ma_), __float_as_uint(ma_), false, false); \
            rm_ = MX3(__uint_as_float(rr_[0]), __uint_as_float(rr_[1]), 0.f); PIN(rm_); } while (0)
#define APPLY_PEND() do { if (__builtin_expect(pend, 0)) { _Pragma("unroll") for (int d = 0; d < 4; ++d) _Pragma("unroll") for (int i = 0; i < 16; ++i) o[d][i] *= fpend; pend = false; } } while (0)
#define DECIDE(SN, PP1, DOPV) do { \
            if (__builtin_expect(__any(rm_ > THR), 0)) { const float dl_ = rm_; \
                _Pragma("unroll") for (int i = 0; i < 16; ++i) { SN[i] -= dl_; negm[i] -= dl_; } \
                fpend = __builtin_amdgcn_exp2f(-dl_); lsum *= fpend; pend = true; } \
        } while (0)
#define SLOTM(SC, SN, PP0, PP1, PC0, PC1, kp, vp, DOQK, DOPV, MID) do { \
            float rm_, ma_, mb_, mc_, md_, me_; f32x2 s0_, s1_; \
            bf16x8 k0_, k1_, k2_, k3_; \
            if (DOQK) { k1_ = KRD(kp, 1); k0_ = KRD(kp, 0); k3_ = KRD(kp, 3); k2_ = KRD(kp, 2); } \
            SB(); \
            APPLY_PEND(); \
              if (DOPV) o[0] = MFMA32(vf0, BC8(PP0), o[0]); RMAX1(SC); SB(); \
              if (DOPV) { o[1] = MFMA32(vf1, BC8(PP0), o[1]); vf1 = VRD(vp, 1, 1); vf0 = VRD(vp, 1, 0); } RMAX2(); SB(); \
              if (DOPV) o[2] = MFMA32(vf2, BC8(PP0), o[2]); SB(); \
              if (DOPV) { o[3] = MFMA32(vf3, BC8(PP0), o[3]); vf3 = VRD(vp, 1, 3); vf2 = VRD(vp, 1, 2); } SB(); \
            DECIDE(SC, PP1, DOPV); SB(); \
              if (DOQK) SN = MFMA32(k0_, qf[0], negm); EX1(SC, 0); EX1(SC, 1); PIN(SC); SB(); \
              if (DOPV) o[0] = MFMA32(vf0, BC8(PP1), o[0]); EX1(SC, 2); EX1(SC, 3); PC0[0] = cvtpk(SC[0], SC[1]); PIN(SC); PIN(PC0); SB(); \
              if (DOQK) { SN = MFMA32(k1_, qf[1], SN); k1_ = KRD(kp, 5); k0_ = KRD(kp, 4); } s0_ = PAIR(SC, 0) + PAIR(SC, 1); PIN2(s0_); EX1(SC, 4); EX1(SC, 5); PIN(SC); SB(); \
              if (DOPV) o[1] = MFMA32(vf1, BC8(PP1), o[1]); PC0[1] = cvtpk(SC[2], SC[3]); EX1(SC, 6); PC0[2] = cvtpk(SC[4], SC[5]); PIN(SC); PIN(PC0); SB(); \
              if (DOQK) SN = MFMA32(k2_, qf[2], SN); s0_ += PAIR(SC, 2); PIN2(s0_); EX1(SC, 7); EX1(SC, 8); PIN(SC); SB(); \
              if (DOPV) o[2] = MFMA32(vf2, BC8(PP1), o[2]); EX1(SC, 9); PC0[3] = cvtpk(SC[6], SC[7]); PIN(SC); PIN(PC0); SB(); \
              if (DOQK) { SN = MFMA32(k3_, qf[3], SN); k3_ = KRD(kp, 7); k2_ = KRD(kp, 6); } s1_ = PAIR(SC, 3) + PAIR(SC, 4); PIN2(s1_); EX1(SC, 10); EX1(SC, 11); PIN(SC); SB(); \
              if (DOPV) o[3] = MFMA32(vf3, BC8(PP1), o[3]); PC1[0] = cvtpk(SC[8], SC[9]); EX1(SC, 12); PC1[1] = cvtpk(SC[10], SC[11]); PIN(SC); PIN(PC1); SB(); \
              if (DOQK) SN = MFMA32(k0_, qf[4], SN); s1_ += PAIR(SC, 5); PIN2(s1_); EX1(SC, 13); EX1(SC, 14); PIN(SC); SB(); \
              if (DOQK) SN = MFMA32(k1_, qf[5], SN); EX1(SC, 15); PC1[2] = cvtpk(SC[12], SC[13]); PIN(SC); PIN(PC1); SB(); \
            MID; SB(); \
              if (DOQK) SN = MFMA32(k2_, qf[6], SN); s0_ += PAIR(SC, 6); s1_ += PAIR(SC, 7); PIN2(s0_); PIN2(s1_); PC1[3] = cvtpk(SC[14], SC[15]); PIN(PC1); SB(); \
              if (DOQK) SN = MFMA32(k3_, qf[7], SN); lsum += s0_; lsum += s1_; PIN2(lsum); SB(); \
        } while (0)
#define VPRE(a0, a1, vp) do { vf1 = VRDX(a0, a1, vp, 0, 1); vf0 = VRDX(a0, a1, vp, 0, 0); vf3 = VRDX(a0, a1, vp, 0, 3); vf2 = VRDX(a0, a1, vp, 0, 2); } while (0)
#define ABAR(Tn) do { LBAR(); } while (0)
        #pragma unroll 1
        for (int r = 0; r < 2; ++r) {
            {
                BASES();
#pragma unroll
                for (int ks = 0; ks < 8; ++ks) sA = MFMA32(KRD(0, ks), qf[ks], ks ? sA : (f32x16{}));
                float rm; { float a_ = MX3(sA[0], sA[1], sA[2]), b_ = MX3(sA[3], sA[4], sA[5]); a_ = MX3(a_, sA[6], sA[7]); b_ = MX3(b_, sA[8], sA[9]); a_ = MX3(a_, sA[10], sA[11]); b_ = MX3(b_, sA[12], sA[13]);
                    a_ = MX3(a_, sA[14], sA[15]); rm = swap32max(__builtin_fmaxf(a_, b_)); }
#pragma unroll
                for (int i = 0; i < 16; ++i) { sA[i] -= rm; negm[i] = -rm; }
                pend = false;
                SLOTM(sA, sB, pB0, pB1, pA0, pA1, 8192, 0, true, false, { if (r == 0) asm volatile("s_waitcnt vmcnt(0)" ::: "memory");
                    ABAR(16 * r + 1); VPRE(n0p, n1p, 0); });
                ADVANCE();
            }
#pragma unroll 1
            for (int t = 1; t < 16; ++t) {
                BASES();
                SLOTM(sB, sA, pA0, pA1, pB0, pB1, 0, 0, true, true, { VPRE(v0p, v1p, 8192); });
                SLOTM(sA, sB, pB0, pB1, pA0, pA1, 8192, 8192, true, true, { ABAR(16 * r + t + 1); VPRE(n0p, n1p, 0); });
                ADVANCE();
            }
            {
                BASES();
                SLOTM(sB, sA, pA0, pA1, pB0, pB1, 0, 0, false, true, { VPRE(v0p, v1p, 8192); });
                APPLY_PEND();
                o[0] = MFMA32(vf0, BC8(pB0), o[0]); o[1] = MFMA32(vf1, BC8(pB0), o[1]); vf1 = VRD(8192, 1, 1); vf0 = VRD(8192, 1, 0);
                o[2] = MFMA32(vf2, BC8(pB0), o[2]); o[3] = MFMA32(vf3, BC8(pB0), o[3]); vf3 = VRD(8192, 1, 3); vf2 = VRD(8192, 1, 2);
                o[0] = MFMA32(vf0, BC8(pB1), o[0]); o[1] = MFMA32(vf1, BC8(pB1), o[1]); o[2] = MFMA32(vf2, BC8(pB1), o[2]); o[3] = MFMA32(vf3, BC8(pB1), o[3]);
                if (r == 0) {
                    ETAG();
                    unsigned qa = lds0 + EX + 4096u * qb + lo16; asm volatile("" : "+v"(qa));
#pragma unroll
                    for (int s = 0; s < 8; ++s) qf[s] = ldsrd128((lds_cptr)(uintptr_t)qa + 16384 * (s >> 2) + 1024 * (s & 3));
                }
                const float inv = 1.0f / swap32sum(lsum[0] + lsum[1]);
                int ln = lane; asm volatile("" : "+v"(ln));
                const unsigned ovo = (unsigned)((ln >> 4) & 1) * 4096u + (unsigned)(ln & 15) * 16u + (unsigned)(ln >> 5) * 256u
                                   + (unsigned)((((g * 32 + h0 + r) * 8 + 2 * qb) * 4)) * 1024u;
                unsigned lo_ = lds0 + EX + 4096u * qb + (unsigned)((ln >> 4) & 1) * 16384u + (unsigned)(ln & 15) * 16u + (unsigned)(ln >> 5) * 256u; asm volatile("" : "+v"(lo_));
#pragma unroll
                for (int db = 0; db < 4; ++db)
#pragma unroll
                    for (int sg = 0; sg < 2; ++sg) { const bf16x8 pk_ = pack8s(o[db], sg, inv);
                        if (r == 0) *(LAS bf16x8*)(uintptr_t)(lo_ + 1024u * db + 512u * sg) = pk_; else { st16_sc1(BCU(pk_), ovo + 1024u * db + 512u * sg, srdO); pk2[2 * db + sg] = pk_; } }
#pragma unroll
                for (int i = 0; i < 4; ++i) o[i] = f32x16{};
                lsum = f32x2{0.f, 0.f};
            }
        }
                LBAR();
#pragma unroll
        for (int i = 0; i < 2; ++i)
#pragma unroll
            for (int c = 0; c < 9; ++c) oacc[i][c] = f32x4{};
    }
    {
        const int mg = w & 3, kq = w >> 2;
        int ln = lane; asm volatile("" : "+v"(ln));
        const unsigned ab0 = lds0 + (unsigned)(16 * ((ln >> 4) >> 1) + 4 * ((ln >> 4) & 1) + ((ln & 15) >> 2)) * 288u + 8u * (ln & 3);
#define TCHUNK(base, F) do { unsigned a0a = (base) + ab0; asm volatile("" : "+v"(a0a)); const lds_cptr kb_ = (lds_cptr)(uintptr_t)a0a; bf16x8 af_[18]; \
            _Pragma("unroll") for (int ks = 0; ks < 2; ++ks) _Pragma("unroll") for (int c = 0; c < 9; ++c) af_[9 * ks + c] = cat8(vtr(kb_ + 9216 * ks + 32 * c), vtr(kb_ + 9216 * ks + 2304 + 32 * c)); \
            SB(); \
            _Pragma("unroll") for (int ks = 0; ks < 2; ++ks) _Pragma("unroll") for (int c = 0; c < 9; ++c) { \
                oacc[0][c] = MFMA16(af_[9 * ks + c], (F)[2 * ks], oacc[0][c]); oacc[1][c] = MFMA16(af_[9 * ks + c], (F)[2 * ks + 1], oacc[1][c]); } \
            SB(); } while (0)
        if (kq == 0) {
            unsigned sw_ = lds0 + 73728u + 4096u * (unsigned)mg + (unsigned)((ln >> 4) & 1) * 2048u + (unsigned)(ln & 15) * 16u + (unsigned)(ln >> 5) * 256u;
            unsigned sr_ = lds0 + 73728u + 4096u * (unsigned)mg + (unsigned)ln * 16u;
            asm volatile("" : "+v"(sw_), "+v"(sr_));
#pragma unroll
            for (int hf = 0; hf < 2; ++hf) {
#pragma unroll
                for (int d = 0; d < 2; ++d)
#pragma unroll
                    for (int sg = 0; sg < 2; ++sg) *(LAS bf16x8*)(uintptr_t)(sw_ + 1024u * d + 512u * sg) = pk2[4 * hf + 2 * d + sg];
                asm volatile("" ::: "memory");
                bf16x8 f_[4];
#pragma unroll
                for (int d = 0; d < 2; ++d)
#pragma unroll
                    for (int m = 0; m < 2; ++m) f_[2 * d + m] = ldsrd128((lds_cptr)(uintptr_t)(sr_ + 2048u * m + 1024u * d));
                asm volatile("" ::: "memory");
                if (hf == 0) TCHUNK(WS1, f_); else TCHUNK(WS2, f_);
                if (hf == 0) {
                    asm volatile("s_waitcnt vmcnt(0)" ::: "memory");
                    if (ln == 0) st4_sc1(tag, WS_FO + (unsigned)((((g * 4 + hq) * 2 + 1) * 4 + mg) * 4 + c4) * 4u, srdF);
                }
            }
        }
        wait_tag(WS_FO + (unsigned)((((g * 4 + hq) * 2 + 1) * 4 + mg) * 4 + ((c4 + 1 + kq + (lane & 1)) & 3)) * 4u, srdF, tag);
                bf16x8 of[12];
#pragma unroll
        for (int i = 0; i < 6; ++i) {
            const int j2 = 4 + 6 * kq + i;
            const int hd = 8 * hq + 2 * ((c4 + (j2 >> 2)) & 3) + 1;
            const unsigned so = (unsigned)((((g * 32 + hd) * 8 + 2 * mg) * 4) + (j2 & 3)) * 1024u;
            LD16_SC1(of[2 * i], lo16, srdO, RFL(so)); LD16_SC1(of[2 * i + 1], lo16, srdO, RFL(so + 4096u));
        }
        asm volatile("s_waitcnt vmcnt(0)\n\ts_barrier" ::: "memory"); SB();
        asm volatile("" : "+v"(of[0]), "+v"(of[1]), "+v"(of[2]), "+v"(of[3]), "+v"(of[4]), "+v"(of[5]), "+v"(of[6]), "+v"(of[7]) :: "memory");
        asm volatile("" : "+v"(of[8]), "+v"(of[9]), "+v"(of[10]), "+v"(of[11]) :: "memory");
                TCHUNK(kq ? WCH : WS0, of); TCHUNK(kq ? 2u * WCH : WS3, of + 4); TCHUNK(kq ? 3u * WCH : 0u, of + 8);
        LBAR();
        const unsigned xw = (unsigned)w * 9216u + lo16, xr = (unsigned)(w ^ 4) * 9216u + lo16;
#pragma unroll
        for (int c = 0; c < 9; ++c) *(LAS f32x4*)(LW + xw + 1024 * c) = kq ? oacc[0][c] : oacc[1][c];
        LBAR();
        f32x4 fin[9];
#pragma unroll
        for (int c = 0; c < 9; ++c) { const f32x4 t4 = *(const LAS f32x4*)(LW + xr + 1024 * c); fin[c] = (kq ? oacc[1][c] : oacc[0][c]) + t4; }
        const int mb = 2 * mg + kq;
        const unsigned lo8 = (unsigned)lane * 8u;
                if (mg != hq) {
            const unsigned sso = (unsigned)((((g * 4 + c4) * 8 + mb) * 4 + hq) * 9) * 512u + lo8;
#pragma unroll
            for (int c = 0; c < 9; ++c) { const u32x2 v2 = {cvtpk(fin[c][0], fin[c][1]), cvtpk(fin[c][2], fin[c][3])}; st8_sc1(v2, sso + 512u * c, srdS); }
            asm volatile("s_waitcnt vmcnt(0)" ::: "memory");
            if (lane == 0) st4_sc1(tag, WS_FS + (unsigned)(((g * 4 + c4) * 8 + mb) * 4 + hq) * 4u, srdF);
        } else {
            const int ot = (hq + 1 + (lane % 3)) & 3;
            wait_tag(WS_FS + (unsigned)(((g * 4 + c4) * 8 + mb) * 4 + ot) * 4u, srdF, tag);
            if (hq == 0 && c4 == 0 && kq == 0 && lane == 0) st4_sc1(tag, WS_E + 64u * (unsigned)g, srdF);
            u32x2 sv[27];
#pragma unroll
            for (int m = 0; m < 3; ++m)
#pragma unroll
                for (int c = 0; c < 9; ++c) LD8_SC1(sv[9 * m + c], lo8, srdS, RFL((unsigned)((((g * 4 + c4) * 8 + mb) * 4 + ((hq + 1 + m) & 3)) * 9 + c) * 512u));
            asm volatile("s_waitcnt vmcnt(0)" : "+v"(sv[0]), "+v"(sv[1]), "+v"(sv[2]), "+v"(sv[3]), "+v"(sv[4]), "+v"(sv[5]), "+v"(sv[6]), "+v"(sv[7]), "+v"(sv[8]) :: "memory");
            asm volatile("" : "+v"(sv[9]), "+v"(sv[10]), "+v"(sv[11]), "+v"(sv[12]), "+v"(sv[13]), "+v"(sv[14]), "+v"(sv[15]), "+v"(sv[16]), "+v"(sv[17]) :: "memory");
            asm volatile("" : "+v"(sv[18]), "+v"(sv[19]), "+v"(sv[20]), "+v"(sv[21]), "+v"(sv[22]), "+v"(sv[23]), "+v"(sv[24]), "+v"(sv[25]), "+v"(sv[26]) :: "memory");
#pragma unroll
            for (int c = 0; c < 9; ++c) {
                f32x4 a = fin[c];
#pragma unroll
                for (int m = 0; m < 3; ++m) { const u32x2 v = sv[9 * m + c];
                    a[0] += __uint_as_float(v[0] << 16); a[1] += __uint_as_float(v[0] & 0xFFFF0000u); a[2] += __uint_as_float(v[1] << 16); a[3] += __uint_as_float(v[1] & 0xFFFF0000u); }
                const u32x2 ov = {cvtpk(a[0], a[1]), cvtpk(a[2], a[3])};
                *(u32x2*)(out + (size_t)(b * 128 + 16 * mb + (lane & 15)) * 1152 + 576 * p + 144 * c4 + 16 * c + 4 * g4) = ov;
            }
        }
    }
    }

extern "C" void kernel_launch(void* const* d_in, const int* in_sizes, int n_in, void* d_out, int out_size, void* d_ws, size_t ws_size, hipStream_t stream) {
    (void)hipFuncSetAttribute((const void*)fused_kernel, hipFuncAttributeMaxDynamicSharedMemorySize, LDS_BYTES);
    hipLaunchKernelGGL(fused_kernel, dim3(256), dim3(512), LDS_BYTES, stream,
                       (const char*)d_in[0], (const char*)d_in[1], (const char*)d_in[2], (const char*)d_in[3], (const char*)d_in[4],
                       (const char*)d_in[5], (const char*)d_in[6], (const char*)d_in[7], (const char*)d_in[8], (const char*)d_in[9],
                       (char*)d_ws, (unsigned short*)d_out);
}
```

```cpp
#include <hip/hip_runtime.h>
#include <hip/hip_bf16.h>
#include <cstdint>

typedef short bf16x8 __attribute__((ext_vector_type(8)));
typedef short s16x4 __attribute__((ext_vector_type(4)));
typedef float f32x16 __attribute__((ext_vector_type(16)));
typedef float f32x4 __attribute__((ext_vector_type(4)));
typedef float f32x2 __attribute__((ext_vector_type(2)));
typedef __bf16 bf16x2_t __attribute__((ext_vector_type(2)));
typedef unsigned u32x4 __attribute__((ext_vector_type(4)));
typedef unsigned u32x2 __attribute__((ext_vector_type(2)));
#define LAS __attribute__((address_space(3)))
#define DI __device__ __forceinline__
typedef LAS const char* lds_cptr;
typedef LAS char* lds_ptr;

constexpr int KVBYTES = 8 * 32 * 1024 * 256;
constexpr unsigned SLOT = 16384;
constexpr int LDS_BYTES = 163840;
constexpr float CSC = 0.08838834764831845f * 1.4426950408889634f;
constexpr float THR = 8.0f;
constexpr unsigned EX = 6 * SLOT, WR = 8 * SLOT;
constexpr unsigned WCH = 18432u, WS0 = 108544u, WS1 = WS0 + WCH, WS2 = WS0 + 2 * WCH, WS3 = WS0 - WCH;
constexpr unsigned WS_E = 0, WS_FO = 4096, WS_FS = 16384;
constexpr size_t WS_O = 65536, WS_O_BYTES = (size_t)2048 * 4096 * 2;
constexpr size_t WS_SL = WS_O + WS_O_BYTES, WS_SL_BYTES = (size_t)16 * 4 * 8 * 4 * 9 * 512;
constexpr unsigned SPIN_CAP = 1u << 20;

#define MFMA32(a, b, c) __builtin_amdgcn_mfma_f32_32x32x16_bf16((a), (b), (c), 0, 0, 0)
#define MFMA16(a, b, c) __builtin_amdgcn_mfma_f32_16x16x32_bf16((a), (b), (c), 0, 0, 0)
#define RFL(x) __builtin_amdgcn_readfirstlane(x)
#define WAITBAR(N) asm volatile("s_waitcnt vmcnt(" #N ") lgkmcnt(0)\n\ts_barrier" ::: "memory")
template <int N> __device__ __forceinline__ void waitbar() { asm volatile("s_waitcnt vmcnt(%0) lgkmcnt(0)\n\ts_barrier" :: "i"(N) : "memory"); }
template <int N> __device__ __forceinline__ void waitv() { asm volatile("s_waitcnt vmcnt(%0)" :: "i"(N) : "memory"); }
#define WB17 WAITBAR(17)
#define WWIN 3
#define XWIN 4
#define LBAR() asm volatile("s_waitcnt lgkmcnt(0)\n\ts_barrier" ::: "memory")
#define SB() __builtin_amdgcn_sched_barrier(0)
#define PIN(x) asm volatile("" : "+v"(x))
#define PIN2(x) asm volatile("" : "+v"(x))

DI unsigned cvtpk(float lo, float hi) { f32x2 v = {lo, hi}; bf16x2_t b = __builtin_convertvector(v, bf16x2_t); return __builtin_bit_cast(unsigned, b); }
DI s16x4 vtr(lds_cptr p) { return __builtin_bit_cast(s16x4, __builtin_amdgcn_ds_read_tr16_b64_v4i16((LAS s16x4*)p)); }
DI bf16x8 cat8(s16x4 a, s16x4 b) { return __builtin_shufflevector(a, b, 0, 1, 2, 3, 4, 5, 6, 7); }
DI bf16x8 ldsrd128(lds_cptr p) { return *(const LAS bf16x8*)p; }
DI float swap32max(float m) { auto rr = __builtin_amdgcn_permlane32_swap(__float_as_uint(m), __float_as_uint(m), false, false); return __builtin_fmaxf(__uint_as_float(rr[0]), __uint_as_float(rr[1])); }
DI float swap32sum(float m) { auto rr = __builtin_amdgcn_permlane32_swap(__float_as_uint(m), __float_as_uint(m), false, false); return __uint_as_float(rr[0]) + __uint_as_float(rr[1]); }
DI bf16x8 pack8s(const f32x16& a, int sg, float sc) {
    u32x4 wv = {cvtpk(a[8 * sg + 0] * sc, a[8 * sg + 1] * sc), cvtpk(a[8 * sg + 2] * sc, a[8 * sg + 3] * sc), cvtpk(a[8 * sg + 4] * sc, a[8 * sg + 5] * sc), cvtpk(a[8 * sg + 6] * sc, a[8 * sg + 7] * sc)};
    return __builtin_bit_cast(bf16x8, wv);
}
#define BC8(x) __builtin_bit_cast(bf16x8, x)
#define BCU(x) __builtin_bit_cast(u32x4, x)

DI void dma16_nt(unsigned ldsdst, unsigned voff, __amdgpu_buffer_rsrc_t srd, unsigned soff) {
    asm volatile("s_mov_b32 m0, %0\n\ts_nop 0\n\tbuffer_load_dwordx4 %1, %2, %3 offen nt lds" :: "s"(ldsdst), "v"(voff), "s"(srd), "s"(soff) : "memory");
}
DI void dma16(unsigned ldsdst, unsigned voff, __amdgpu_buffer_rsrc_t srd, unsigned soff) {
    asm volatile("s_mov_b32 m0, %0\n\ts_nop 0\n\tbuffer_load_dwordx4 %1, %2, %3 offen lds" :: "s"(ldsdst), "v"(voff), "s"(srd), "s"(soff) : "memory");
}
DI void st16_sc1(u32x4 v, unsigned voff, __amdgpu_buffer_rsrc_t srd) { __builtin_amdgcn_raw_buffer_store_b128(v, srd, (int)voff, 0, 16); }
DI void st8_sc1(u32x2 v, unsigned voff, __amdgpu_buffer_rsrc_t srd) { __builtin_amdgcn_raw_buffer_store_b64(v, srd, (int)voff, 0, 16); }
DI void st4_sc1(unsigned v, unsigned voff, __amdgpu_buffer_rsrc_t srd) { __builtin_amdgcn_raw_buffer_store_b32(v, srd, (int)voff, 0, 16); }
#define LD16_SC1(dst, voff, srd, soff) asm volatile("buffer_load_dwordx4 %0, %1, %2, %3 offen sc1" : "=v"(dst) : "v"(voff), "s"(srd), "s"(soff) : "memory")
#define LD8_SC1(dst, voff, srd, soff) asm volatile("buffer_load_dwordx2 %0, %1, %2, %3 offen sc1" : "=v"(dst) : "v"(voff), "s"(srd), "s"(soff) : "memory")
#define LD4_SC1(dst, voff, srd) asm volatile("buffer_load_dword %0, %1, %2, 0 offen sc1" : "=v"(dst) : "v"(voff), "s"(srd) : "memory")
#define LD16(dst, voff, srd, soff) asm volatile("buffer_load_dwordx4 %0, %1, %2, %3 offen" : "=v"(dst) : "v"(voff), "s"(srd), "s"(soff) : "memory")
DI void wait_tag(unsigned voff, __amdgpu_buffer_rsrc_t srd, unsigned tag) {
    for (unsigned sp = 0; sp < SPIN_CAP; ++sp) {
        unsigned f;
        asm volatile("buffer_load_dword %0, %1, %2, 0 offen sc1\n\ts_waitcnt vmcnt(0)" : "=v"(f) : "v"(voff), "s"(srd) : "memory");
        if (__all(f == tag)) break;
        __builtin_amdgcn_s_sleep(1);
    }
}

__global__ __launch_bounds__(512, 2) void fused_kernel(const char* __restrict__ xA, const char* __restrict__ kA, const char* __restrict__ vA, const char* __restrict__ wqA, const char* __restrict__ woA,
                                                      const char* __restrict__ xB, const char* __restrict__ kB, const char* __restrict__ vB, const char* __restrict__ wqB, const char* __restrict__ woB,
                                                      char* __restrict__ ws, unsigned short* __restrict__ out) {
    extern __shared__ __attribute__((aligned(16))) char lds[];
    const int tid = threadIdx.x, lane = tid & 63;
    const int w = RFL(tid >> 6);
    const int r32 = lane & 31, hi = lane >> 5;
    const int wg = blockIdx.x, xcd = wg & 7, idx = wg >> 3;
    const int p = xcd & 1, hq = xcd >> 1, c4 = idx & 3, b = idx >> 2;
    const int g = p * 8 + b;
    const int h0 = 8 * hq + 2 * c4;
    const unsigned lds0 = (unsigned)(uintptr_t)lds;
    const lds_cptr L = (lds_cptr)lds;
    const lds_ptr LW = (lds_ptr)lds;
        const __amdgpu_buffer_rsrc_t srdX = __builtin_amdgcn_make_buffer_rsrc((void*)(p ? xB : xA), (short)0, 8 * 128 * 576 * 2, 0x00020000);
    const __amdgpu_buffer_rsrc_t srdW = __builtin_amdgcn_make_buffer_rsrc((void*)(p ? wqB : wqA), (short)0, 576 * 4096 * 2, 0x00020000);
    const __amdgpu_buffer_rsrc_t srdK = __builtin_amdgcn_make_buffer_rsrc((void*)(p ? kB : kA), (short)0, KVBYTES, 0x00020000);
    const __amdgpu_buffer_rsrc_t srdV = __builtin_amdgcn_make_buffer_rsrc((void*)(p ? vB : vA), (short)0, KVBYTES, 0x00020000);
    const __amdgpu_buffer_rsrc_t srdWo = __builtin_amdgcn_make_buffer_rsrc((void*)(p ? woB : woA), (short)0, 4096 * 576 * 2, 0x00020000);
    const __amdgpu_buffer_rsrc_t srdO = __builtin_amdgcn_make_buffer_rsrc((void*)(ws + WS_O), (short)0, (int)WS_O_BYTES, 0x00020000);
    const __amdgpu_buffer_rsrc_t srdS = __builtin_amdgcn_make_buffer_rsrc((void*)(ws + WS_SL), (short)0, (int)WS_SL_BYTES, 0x00020000);
    const __amdgpu_buffer_rsrc_t srdF = __builtin_amdgcn_make_buffer_rsrc((void*)ws, (short)0, 65536, 0x00020000);

    const bool isB = w >= 4;
    const int bw = w & 3;
    const int r7 = (lane >> 2) & 7;
    const int blk = (lane >> 4) & 1, q4 = (lane & 15) >> 2, p4 = lane & 3, g4 = lane >> 4;
    const unsigned lo16 = (unsigned)lane * 16u;
    unsigned ev;
#define ELOAD() do { const unsigned eo_ = WS_E + 64u * (unsigned)g; asm volatile("buffer_load_dword %0, %1, %2, 0 offen sc1" : "=v"(ev) : "v"(eo_), "s"(srdF) : "memory"); } while (0)
#define ETAG() do { PIN(ev); tag = (unsigned)RFL(ev) * 0x9E3779B1u + 0x7F4A7C15u; } while (0)
    const unsigned chs0 = 16u * (4u * (unsigned)hi + (unsigned)((lane & 3) ^ ((2 * (2 * bw) + (r7 >> 2)) & 3)));
    const unsigned chs1 = 16u * (4u * (unsigned)hi + (unsigned)((lane & 3) ^ ((2 * (2 * bw + 1) + (r7 >> 2)) & 3)));
    const unsigned srcKV0 = (unsigned)(16 * bw + r7) * 256u + chs0, srcKV1 = (unsigned)(16 * bw + 8 + r7) * 256u + chs1;
    const unsigned srcW0 = (unsigned)(16 * bw + r7) * 8192u + chs0, srcW1 = (unsigned)(16 * bw + 8 + r7) * 8192u + chs1;
    auto kv_off = [&](int T) -> unsigned { return (unsigned)((b * 32 + h0) * 1024 + 64 * T) * 256u; };
    auto kslot = [&](int T) -> unsigned { return (unsigned)(T % 3) * SLOT; };
    auto vslot = [&](int T) -> unsigned { return (unsigned)(3 + T % 3) * SLOT; };
    auto dma_tile = [&](__amdgpu_buffer_rsrc_t srd, int T, unsigned slot) { const unsigned so = kv_off(T), d = lds0 + 4096u * (unsigned)bw + slot;
        dma16_nt(RFL(d), srcKV0, srd, RFL(so)); dma16_nt(RFL(d + 1024), srcKV0, srd, RFL(so + 128));
        dma16_nt(RFL(d + 2048), srcKV1, srd, RFL(so)); dma16_nt(RFL(d + 3072), srcKV1, srd, RFL(so + 128)); };
    auto dma_k = [&](int T) { dma_tile(srdK, T, kslot(T)); };
    auto dma_v = [&](int T) { dma_tile(srdV, T, vslot(T)); };
    const int rot = (b + 2 * c4) % 9;
    auto dma_wjob = [&](int head, int n, unsigned dst) {
        const int ps = n / 9, kc0 = n - 9 * ps + rot, kc = kc0 >= 9 ? kc0 - 9 : kc0;
        const unsigned sw = (unsigned)(64 * kc) * 8192u + (unsigned)head * 256u + 128u * (unsigned)ps, d = lds0 + dst + 2048u * (unsigned)bw;
        dma16(RFL(d), srcW0, srdW, RFL(sw)); dma16(RFL(d + 1024), srcW1, srdW, RFL(sw));
    };
    auto dma_wdj = [&](int head, int gj, unsigned dst) { dma_wjob(head, 2 * gj, dst); dma_wjob(head, 2 * gj + 1, dst + 8192u); };
    unsigned tag;

    bf16x8 pk2[8];
    f32x4 oacc[2][9];
    if (isB) {
        bf16x8 xf[36];
                const unsigned vx = (unsigned)(32 * bw + (lane >> 3)) * 1152u + 16u * (lane & 7);
        const unsigned sx = (unsigned)(b * 128) * 1152u;
#define XLOAD(c) do { const int kk_ = (c) + rot; const unsigned so_ = sx + 128u * (unsigned)(kk_ >= 9 ? kk_ - 9 : kk_); \
            LD16(xf[4 * (c)], vx, srdX, RFL(so_)); LD16(xf[4 * (c) + 1], vx, srdX, RFL(so_ + 9216u)); LD16(xf[4 * (c) + 2], vx, srdX, RFL(so_ + 18432u)); LD16(xf[4 * (c) + 3], vx, srdX, RFL(so_ + 27648u)); } while (0)
        unsigned tch = 0;
#define TOUCH(vo, srd, so) asm volatile("buffer_load_dword %0, %1, %2, %3 offen" : "+v"(tch) : "v"(vo), "s"(srd), "s"(so) : "memory")
#define TOUCH_WQ() do { int l_ = lane < 36 ? lane : 35; asm volatile("" : "+v"(l_)); const unsigned so_ = (unsigned)(72 * b + 18 * bw) * 8192u + (unsigned)(h0 + 1) * 256u; \
            TOUCH((unsigned)(l_ >> 1) * 8192u + 128u * (l_ & 1), srdW, RFL(so_)); } while (0)
#define TOUCH_WO(r) do { int l_ = lane < 36 ? lane : 35; asm volatile("" : "+v"(l_)); const unsigned so_ = (unsigned)(128 * (h0 + (r)) + 16 * b + 4 * bw) * 1152u; \
            TOUCH((unsigned)l_ * 128u, srdWo, RFL(so_)); } while (0)
        XLOAD(0); XLOAD(1); XLOAD(2); XLOAD(3);
        TOUCH_WO(0);
        if (XWIN > 4) XLOAD(4); if (XWIN > 5) XLOAD(5); if (XWIN > 6) XLOAD(6); if (XWIN > 7) XLOAD(7); if (XWIN > 8) XLOAD(8);
        const unsigned xwe = lds0 + EX + 4096u * bw + (unsigned)(lane >> 3) * 128u + 16u * (unsigned)((lane & 7) ^ ((lane >> 4) & 7));
        const unsigned xwo = lds0 + EX + 4096u * bw + (unsigned)(8 + (lane >> 3)) * 128u + 16u * (unsigned)((lane & 7) ^ ((4 + (lane >> 4)) & 7));
        const unsigned xrd = lds0 + EX + 4096u * bw + (unsigned)r32 * 128u + 16u * (unsigned)(hi ^ ((r32 >> 1) & 7));
        const unsigned wb0 = lds0 + 1024u * hi + 64u * (0 + q4) + 16u * ((2 * blk + (p4 & 1)) ^ (2 * hi + 0)) + 8u * (p4 >> 1);
        const unsigned wb1 = lds0 + 1024u * hi + 64u * (4 + q4) + 16u * ((2 * blk + (p4 & 1)) ^ (2 * hi + 1)) + 8u * (p4 >> 1);
        const unsigned qex = EX + 4096u * bw + lo16;
        f32x16 acc[2];
        bf16x8 ar[8];
#define XCONV(c) do { asm volatile("s_waitcnt vmcnt(%4)" : "+v"(xf[4 * (c)]), "+v"(xf[4 * (c) + 1]), "+v"(xf[4 * (c) + 2]), "+v"(xf[4 * (c) + 3]) : "i"(4 * ((((c) - 1 + XWIN < 8) ? (c) - 1 + XWIN : 8) - (c)) + (((c) + XWIN > 8 || (c) < XWIN) ? 1 : 0)) : "memory"); \
            *(LAS bf16x8*)(uintptr_t)(xwe) = xf[4 * (c)]; *(LAS bf16x8*)(uintptr_t)(xwo) = xf[4 * (c) + 1]; *(LAS bf16x8*)(uintptr_t)(xwe + 2048) = xf[4 * (c) + 2]; *(LAS bf16x8*)(uintptr_t)(xwo + 2048) = xf[4 * (c) + 3]; \
            asm volatile("" ::: "memory"); \
            _Pragma("unroll") for (int ks = 0; ks < 4; ++ks) xf[4 * (c) + ks] = ldsrd128((lds_cptr)(uintptr_t)(xrd ^ (32u * ks))); \
            asm volatile("" ::: "memory"); \
            if ((c) + XWIN <= 8) XLOAD(((c) + XWIN <= 8) ? (c) + XWIN : 0); if ((c) + XWIN == 8) { TOUCH_WQ(); } } while (0)
#define WFR(wp0, wp1, m) cat8(vtr((wp0) + 8192 * ((m) >> 3) + 2048 * (((m) >> 1) & 3) + 512 * ((m) & 1)), vtr((wp1) + 8192 * ((m) >> 3) + 2048 * (((m) >> 1) & 3) + 512 * ((m) & 1)))
#define WPTR(p0, p1, base) unsigned p0##a = wb0 + (base), p1##a = wb1 + (base); asm volatile("" : "+v"(p0##a), "+v"(p1##a)); const lds_cptr p0 = (lds_cptr)(uintptr_t)p0##a, p1 = (lds_cptr)(uintptr_t)p1##a
#define QMF(gj, m) do { constexpr int n_ = 2 * (gj) + ((m) >> 3), kc_ = n_ % 9, ks_ = ((m) >> 1) & 3, dl_ = (m) & 1; \
            if (kc_ == 0 && ks_ == 0) acc[dl_] = MFMA32(ar[(m) & 7], xf[4 * kc_ + ks_], (f32x16{})); else acc[dl_] = MFMA32(ar[(m) & 7], xf[4 * kc_ + ks_], acc[dl_]); \
            PIN(acc[dl_]); \
            if (kc_ == 8 && ((m) & 7) == 7) { _Pragma("unroll") for (int dl = 0; dl < 2; ++dl) _Pragma("unroll") for (int sg = 0; sg < 2; ++sg) \
                *(LAS bf16x8*)(LW + qex + 16384u * (n_ / 9) + (2 * dl + sg) * 1024) = pack8s(acc[dl], sg, CSC); } } while (0)
#define QSTREAM(gj, base, MID, HASNEXT, nbase) do { \
            { WPTR(c0, c1, base); \
              QMF(gj, 0); ar[6] = WFR(c0, c1, 6); SB(); QMF(gj, 1); ar[7] = WFR(c0, c1, 7); SB(); QMF(gj, 2); ar[0] = WFR(c0, c1, 8); SB(); QMF(gj, 3); ar[1] = WFR(c0, c1, 9); SB(); \
              QMF(gj, 4); ar[2] = WFR(c0, c1, 10); SB(); QMF(gj, 5); ar[3] = WFR(c0, c1, 11); SB(); QMF(gj, 6); ar[4] = WFR(c0, c1, 12); SB(); QMF(gj, 7); ar[5] = WFR(c0, c1, 13); SB(); \
              QMF(gj, 8); ar[6] = WFR(c0, c1, 14); SB(); QMF(gj, 9); ar[7] = WFR(c0, c1, 15); SB(); QMF(gj, 10); SB(); QMF(gj, 11); SB(); } \
            MID; SB(); \
            if (HASNEXT) { WPTR(n0, n1, nbase); \
              QMF(gj, 12); ar[0] = WFR(n0, n1, 0); ar[1] = WFR(n0, n1, 1); SB(); QMF(gj, 13); ar[2] = WFR(n0, n1, 2); ar[3] = WFR(n0, n1, 3); SB(); \
              QMF(gj, 14); SB(); QMF(gj, 15); ar[4] = WFR(n0, n1, 4); ar[5] = WFR(n0, n1, 5); SB(); } \
            else { QMF(gj, 12); SB(); QMF(gj, 13); SB(); QMF(gj, 14); SB(); QMF(gj, 15); SB(); } \
        } while (0)
#define QDJ(T, gj, base) do { WPTR(c0, c1, base); \
            dma_k((T) + 2); \
            _Pragma("unroll") for (int m = 0; m < 8; ++m) ar[m] = WFR(c0, c1, m); SB(); \
            QMF(gj, 0); QMF(gj, 1); QMF(gj, 2); QMF(gj, 3); QMF(gj, 4); QMF(gj, 5); QMF(gj, 6); QMF(gj, 7); SB(); \
            dma_v((T) + 1); \
            _Pragma("unroll") for (int m = 8; m < 16; ++m) ar[m & 7] = WFR(c0, c1, m); SB(); \
            QMF(gj, 8); QMF(gj, 9); QMF(gj, 10); QMF(gj, 11); QMF(gj, 12); QMF(gj, 13); QMF(gj, 14); QMF(gj, 15); SB(); } while (0)
                XCONV(0); XCONV(1);
        LBAR();         { WPTR(c0, c1, 0u); ar[0] = WFR(c0, c1, 0); ar[1] = WFR(c0, c1, 1); ar[2] = WFR(c0, c1, 2); ar[3] = WFR(c0, c1, 3); ar[4] = WFR(c0, c1, 4); ar[5] = WFR(c0, c1, 5); }
        QSTREAM(0, 0u, LBAR(), true, SLOT);     XCONV(2); XCONV(3);         QSTREAM(1, SLOT, LBAR(), true, 3 * SLOT);          XCONV(4); XCONV(5);
        QSTREAM(2, 3 * SLOT, LBAR(), true, 2 * SLOT);      XCONV(6); XCONV(7);
        QSTREAM(3, 2 * SLOT, LBAR(), true, 4 * SLOT);      XCONV(8);
        QSTREAM(4, 4 * SLOT, LBAR(), true, 5 * SLOT);         QSTREAM(5, 5 * SLOT, LBAR(), true, EX + SLOT);
        QSTREAM(6, EX + SLOT, LBAR(), true, WR);
        QSTREAM(7, WR, LBAR(), true, WR + SLOT);
        QSTREAM(8, WR + SLOT, {}, false, 0u);
                LBAR();
#define BKV(T) do { if ((T) < 30) dma_k((T) + 2); if ((T) < 31) dma_v((T) + 1); } while (0)
        BKV(0); dma_wdj(h0 + 1, 0, WR); dma_wdj(h0 + 1, 1, WR + SLOT); WAITBAR(16);
        BKV(1); ELOAD(); WAITBAR(9);
        QDJ(2, 0, WR); WAITBAR(8);
        QDJ(3, 1, WR + SLOT); ETAG(); dma_wdj(h0 + 1, 2, WR); WAITBAR(12);
        BKV(4); dma_wdj(h0 + 1, 3, WR + SLOT); WAITBAR(12);
        QDJ(5, 2, WR); WAITBAR(8);
        QDJ(6, 3, WR + SLOT); dma_wdj(h0 + 1, 4, WR); WAITBAR(12);
        BKV(7); dma_wdj(h0 + 1, 5, WR + SLOT); WAITBAR(12);
        QDJ(8, 4, WR); WAITBAR(8);
        QDJ(9, 5, WR + SLOT); dma_wdj(h0 + 1, 6, WR); WAITBAR(12);
        BKV(10); dma_wdj(h0 + 1, 7, WR + SLOT); WAITBAR(12);
        QDJ(11, 6, WR); WAITBAR(8);
        QDJ(12, 7, WR + SLOT); dma_wdj(h0 + 1, 8, WR); WAITBAR(12);
        BKV(13); TOUCH_WO(1); WAITBAR(9);
        QDJ(14, 8, WR); WAITBAR(8);
        BKV(15); WAITBAR(8);
        {
            int ln = lane; asm volatile("" : "+v"(ln));
            unsigned wov[5];
#pragma unroll
            for (int i = 0; i < 5; ++i) {
                const int pc = (i < 4) ? bw + 4 * i : 16 + (bw & 1);
                const int uu = 64 * pc + ln, pos = uu / 18, ch = uu - 18 * pos;
                const int k = (pos & ~12) | ((pos & 4) << 1) | ((pos & 8) >> 1);
                wov[i] = (unsigned)k * 1152u + 16u * ch;
            }
            const unsigned ab0 = lds0 + (unsigned)(16 * ((ln >> 4) >> 1) + 4 * ((ln >> 4) & 1) + ((ln & 15) >> 2)) * 288u + 8u * (ln & 3);
            auto dma_wo = [&](int r, int jj, unsigned dst) {
                const int hd = 8 * hq + 2 * ((c4 + (jj >> 1)) & 3) + r;
                const unsigned so = (unsigned)(128 * hd + 64 * (jj & 1)) * 1152u + 288u * c4;
#pragma unroll
                for (int i = 0; i < 5; ++i) { const int pc = (i < 4) ? bw + 4 * i : 16 + (bw & 1); dma16(RFL(lds0 + dst + 1024u * pc), wov[i], srdWo, RFL(so)); }
            };
#pragma unroll
            for (int i = 0; i < 2; ++i)
#pragma unroll
                for (int c = 0; c < 9; ++c) oacc[i][c] = f32x4{};
            bf16x8 ofx[4], ofy[4];
            unsigned flagv;
            const unsigned fo1 = WS_FO + (unsigned)((((g * 4 + hq) * 2 + 0) * 4 + bw) * 4 + (ln & 3)) * 4u;
#define O1L(OF, jj) do { const int hd_ = 8 * hq + 2 * ((c4 + ((jj) >> 1)) & 3); const unsigned so_ = (unsigned)((((g * 32 + hd_) * 8 + 2 * bw) * 4) + 2 * ((jj) & 1)) * 1024u; \
                LD16_SC1(OF[0], lo16, srdO, RFL(so_)); LD16_SC1(OF[1], lo16, srdO, RFL(so_ + 4096u)); LD16_SC1(OF[2], lo16, srdO, RFL(so_ + 1024u)); LD16_SC1(OF[3], lo16, srdO, RFL(so_ + 5120u)); } while (0)
#define OPHALF(base, OF, ks) do { \
                unsigned a0a = ab0 + (base) + 9216u * (ks); asm volatile("" : "+v"(a0a)); \
                const lds_cptr kb_ = (lds_cptr)(uintptr_t)a0a; bf16x8 af_[9]; \
                _Pragma("unroll") for (int c = 0; c < 9; ++c) af_[c] = cat8(vtr(kb_ + 32 * c), vtr(kb_ + 2304 + 32 * c)); \
                SB(); \
                _Pragma("unroll") for (int c = 0; c < 9; ++c) { oacc[0][c] = MFMA16(af_[c], OF[2 * (ks)], oacc[0][c]); oacc[1][c] = MFMA16(af_[c], OF[2 * (ks) + 1], oacc[1][c]); PIN(oacc[0][c]); PIN(oacc[1][c]); } \
                SB(); \
            } while (0)
#define WSL(jj) ((jj) % 3 == 0 ? WS2 : (jj) % 3 == 1 ? WS0 : WS1)
#define ISSUE1(jj, OF) do { dma_wo(0, jj, WSL(jj)); O1L(OF, jj); } while (0)
#define BKVC(T, jj, OF) do { asm volatile("" : "+v"(OF[0]), "+v"(OF[1]), "+v"(OF[2]), "+v"(OF[3]) :: "memory"); \
                if ((T) < 30) dma_k((T) + 2); OPHALF(WSL(jj), OF, 0); if ((T) < 31) dma_v((T) + 1); OPHALF(WSL(jj), OF, 1); } while (0)
            BKV(16); WAITBAR(8);
            {
                unsigned oa = lds0 + EX + 4096u * bw + lo16; asm volatile("" : "+v"(oa)); const lds_cptr op = (lds_cptr)(uintptr_t)oa;
                ofx[0] = ldsrd128(op); ofx[1] = ldsrd128(op + 16384); ofx[2] = ldsrd128(op + 1024); ofx[3] = ldsrd128(op + 17408);
                ofy[0] = ldsrd128(op + 2048); ofy[1] = ldsrd128(op + 18432); ofy[2] = ldsrd128(op + 3072); ofy[3] = ldsrd128(op + 19456); }
            BKV(17); dma_wo(0, 0, WSL(0));
            {   const unsigned so_ = (unsigned)((((g * 32 + h0) * 8 + 2 * bw) * 4)) * 1024u + lo16;
#pragma unroll
                for (int i = 0; i < 4; ++i) { st16_sc1(BCU(ofx[i]), so_ + 4096u * (i & 1) + 1024u * (i >> 1), srdO); st16_sc1(BCU(ofy[i]), so_ + 4096u * (i & 1) + 1024u * (2 + (i >> 1)), srdO); } }
            WAITBAR(21);
            BKV(18); dma_wo(0, 1, WSL(1)); WAITBAR(13);
            if (ln == 0) st4_sc1(tag, WS_FO + (unsigned)((((g * 4 + hq) * 2 + 0) * 4 + bw) * 4 + c4) * 4u, srdF);
            BKVC(19, 0, ofx); WAITBAR(8);
            BKVC(20, 1, ofy); WAITBAR(8);
            LD4_SC1(flagv, fo1, srdF);
            BKV(21); WAITBAR(8);
            BKV(22);
            asm volatile("" : "+v"(flagv) :: "memory");
            if (!__all(flagv == tag)) wait_tag(fo1, srdF, tag);
            ISSUE1(2, ofx); WB17; asm volatile("" :: "v"(tch));
            BKV(23); ISSUE1(3, ofy); WB17;
            BKVC(24, 2, ofx); ISSUE1(4, ofx); WB17;
            BKVC(25, 3, ofy); ISSUE1(5, ofy); WB17;
            BKVC(26, 4, ofx); ISSUE1(6, ofx); WB17;
            BKVC(27, 5, ofy); ISSUE1(7, ofy); WB17;
            BKVC(28, 6, ofx); dma_wo(1, 0, WS1); WAITBAR(13);
            BKVC(29, 7, ofy); dma_wo(1, 1, WS2); WAITBAR(13);
            BKV(30); dma_wo(1, 2, WS0); WAITBAR(9);
            BKV(31); dma_wo(1, 3, WS3); WAITBAR(5);
                                    WAITBAR(0);
            dma_wo(1, 4, 0u); dma_wo(1, 5, WCH); dma_wo(1, 6, 2u * WCH);
            dma_wo(1, 7, 3u * WCH);
        }
    } else {
        {
            constexpr unsigned PS[9] = {0u, SLOT, 3 * SLOT, 2 * SLOT, 4 * SLOT, 5 * SLOT, EX + SLOT, WR, WR + SLOT};
#pragma unroll
            for (int s = 0; s < WWIN; ++s) dma_wdj(h0, s, PS[s]);
            #define PSTEP(s) do { waitbar<4 * (((s) - 1 + WWIN < 8 ? (s) - 1 + WWIN : 8) - (s)) + ((s) == 7 ? 4 : (s) == 8 ? 12 : 0)>(); \
                if ((s) + WWIN <= 8) dma_wdj(h0, ((s) + WWIN <= 8) ? (s) + WWIN : 0, PS[((s) + WWIN <= 8) ? (s) + WWIN : 0]); } while (0)
            PSTEP(0); PSTEP(1); PSTEP(2); PSTEP(3); PSTEP(4); PSTEP(5);
            PSTEP(6); dma_k(0);
            PSTEP(7); dma_k(1); dma_v(0);
            PSTEP(8);
            WAITBAR(8);
            ELOAD();
        }
        const int qb = w;
        bf16x8 qf[8];
        { unsigned qa = lds0 + EX + 4096u * qb + lo16; asm volatile("" : "+v"(qa));
#pragma unroll
          for (int s = 0; s < 8; ++s) qf[s] = ldsrd128((lds_cptr)(uintptr_t)qa + 16384 * (s >> 2) + 1024 * (s & 3)); }
        const unsigned kb0 = 2048u * (r32 >> 3) + 64u * (r32 & 7) + 16u * ((0 + hi) ^ ((r32 >> 2) & 3));
        const unsigned kb1 = 2048u * (r32 >> 3) + 64u * (r32 & 7) + 16u * ((2 + hi) ^ ((r32 >> 2) & 3));
        const unsigned vb0 = 64u * (4 * hi + q4) + 16u * ((2 * blk + (p4 & 1)) ^ (0 + hi)) + 8u * (p4 >> 1);
        const unsigned vb1 = 2048u + 64u * (4 * hi + q4) + 16u * ((2 * blk + (p4 & 1)) ^ (2 + hi)) + 8u * (p4 >> 1);
        f32x16 o[4];
#pragma unroll
        for (int i = 0; i < 4; ++i) o[i] = f32x16{};
        f32x2 lsum = {0.f, 0.f};
        float fpend = 1.f; bool pend = false;
        f32x16 sA = f32x16{}, sB = f32x16{}, negm = f32x16{};
        u32x4 pA0 = {}, pA1 = {}, pB0 = {}, pB1 = {};
        bf16x8 vf0 = {}, vf1 = {}, vf2 = {}, vf3 = {};
        const unsigned kbb0 = lds0 + kb0, kbb1 = lds0 + kb1, vbb0 = lds0 + vb0 + 3 * SLOT, vbb1 = lds0 + vb1 + 3 * SLOT;
        unsigned scur = 0, sprev = 2 * SLOT;
#define KRD(kp, ks) ldsrd128((((ks) & 1) ? k1p : k0p) + (kp) + 512 * ((ks) >> 1))
#define VRDX(a0, a1, vp, sg, db) cat8(vtr((a0) + (vp) + 4096 * (sg) + 512 * (db)), vtr((a1) + (vp) + 4096 * (sg) + 512 * (db)))
#define VRD(vp, sg, db) VRDX(v0p, v1p, vp, sg, db)
#define BASES() asm volatile("" : "+s"(scur), "+s"(sprev)); \
            unsigned k0a = kbb0 + scur, k1a = kbb1 + scur, v0a = vbb0 + sprev, v1a = vbb1 + sprev, n0a = vbb0 + scur, n1a = vbb1 + scur; \
            asm volatile("" : "+v"(k0a), "+v"(k1a), "+v"(v0a), "+v"(v1a), "+v"(n0a), "+v"(n1a)); \
            const lds_cptr k0p = (lds_cptr)(uintptr_t)k0a, k1p = (lds_cptr)(uintptr_t)k1a, v0p = (lds_cptr)(uintptr_t)v0a, v1p = (lds_cptr)(uintptr_t)v1a, n0p = (lds_cptr)(uintptr_t)n0a, n1p = (lds_cptr)(uintptr_t)n1a
#define ADVANCE() do { sprev = scur; scur = (scur == 2 * SLOT) ? 0u : scur + SLOT; } while (0)
#define EX1(S, i) do { S[i] = __builtin_amdgcn_exp2f(S[i]); } while (0)
#define MX3(a, b, c) __builtin_fmaxf(__builtin_fmaxf(a, b), c)
#define AMAX3(d, a, b, c) asm volatile("v_max3_f32 %0, %1, %2, %3" : "=v"(d) : "v"(a), "v"(b), "v"(c))
#define AMAX2(d, a, b) asm volatile("v_max_f32 %0, %1, %2" : "=v"(d) : "v"(a), "v"(b))
#define PAIR(S, i) (f32x2{S[2 * (i)], S[2 * (i) + 1]})
#define RMAX1(S) ma_ = MX3(S[0], S[1], S[2]); mb_ = MX3(S[3], S[4], S[5]); mc_ = MX3(S[6], S[7], S[8]); md_ = MX3(S[9], S[10], S[11]); me_ = MX3(S[12], S[13], S[14]); \
            ma_ = MX3(ma_, mb_, mc_); md_ = MX3(md_, me_, S[15]); ma_ = MX3(ma_, md_, 0.f); PIN(ma_)
#define RMAX2() do { auto rr_ = __builtin_amdgcn_permlane32_swap(__float_as_uint(ma_), __float_as_uint(ma_), false, false); \
            rm_ = MX3(__uint_as_float(rr_[0]), __uint_as_float(rr_[1]), 0.f); PIN(rm_); } while (0)
#define APPLY_PEND() do { if (__builtin_expect(pend, 0)) { _Pragma("unroll") for (int d = 0; d < 4; ++d) _Pragma("unroll") for (int i = 0; i < 16; ++i) o[d][i] *= fpend; pend = false; } } while (0)
#define DECIDE(SN, PP1, DOPV) do { \
            if (__builtin_expect(__any(rm_ > THR), 0)) { const float dl_ = rm_; \
                _Pragma("unroll") for (int i = 0; i < 16; ++i) { SN[i] -= dl_; negm[i] -= dl_; } \
                fpend = __builtin_amdgcn_exp2f(-dl_); lsum *= fpend; pend = true; } \
        } while (0)
#define SLOTM(SC, SN, PP0, PP1, PC0, PC1, kp, vp, DOQK, DOPV, MID) do { \
            float rm_, ma_, mb_, mc_, md_, me_; f32x2 s0_, s1_; \
            bf16x8 k0_, k1_, k2_, k3_; \
            if (DOQK) { k1_ = KRD(kp, 1); k0_ = KRD(kp, 0); k3_ = KRD(kp, 3); k2_ = KRD(kp, 2); } \
            SB(); \
            APPLY_PEND(); \
              if (DOPV) o[0] = MFMA32(vf0, BC8(PP0), o[0]); RMAX1(SC); SB(); \
              if (DOPV) { o[1] = MFMA32(vf1, BC8(PP0), o[1]); vf1 = VRD(vp, 1, 1); vf0 = VRD(vp, 1, 0); } RMAX2(); SB(); \
              if (DOPV) o[2] = MFMA32(vf2, BC8(PP0), o[2]); SB(); \
              if (DOPV) { o[3] = MFMA32(vf3, BC8(PP0), o[3]); vf3 = VRD(vp, 1, 3); vf2 = VRD(vp, 1, 2); } SB(); \
            DECIDE(SC, PP1, DOPV); SB(); \
              if (DOQK) SN = MFMA32(k0_, qf[0], negm); EX1(SC, 0); EX1(SC, 1); PIN(SC); SB(); \
              if (DOPV) o[0] = MFMA32(vf0, BC8(PP1), o[0]); EX1(SC, 2); EX1(SC, 3); PC0[0] = cvtpk(SC[0], SC[1]); PIN(SC); PIN(PC0); SB(); \
              if (DOQK) { SN = MFMA32(k1_, qf[1], SN); k1_ = KRD(kp, 5); k0_ = KRD(kp, 4); } s0_ = PAIR(SC, 0) + PAIR(SC, 1); PIN2(s0_); EX1(SC, 4); EX1(SC, 5); PIN(SC); SB(); \
              if (DOPV) o[1] = MFMA32(vf1, BC8(PP1), o[1]); PC0[1] = cvtpk(SC[2], SC[3]); EX1(SC, 6); PC0[2] = cvtpk(SC[4], SC[5]); PIN(SC); PIN(PC0); SB(); \
              if (DOQK) SN = MFMA32(k2_, qf[2], SN); s0_ += PAIR(SC, 2); PIN2(s0_); EX1(SC, 7); EX1(SC, 8); PIN(SC); SB(); \
              if (DOPV) o[2] = MFMA32(vf2, BC8(PP1), o[2]); EX1(SC, 9); PC0[3] = cvtpk(SC[6], SC[7]); PIN(SC); PIN(PC0); SB(); \
              if (DOQK) { SN = MFMA32(k3_, qf[3], SN); k3_ = KRD(kp, 7); k2_ = KRD(kp, 6); } s1_ = PAIR(SC, 3) + PAIR(SC, 4); PIN2(s1_); EX1(SC, 10); EX1(SC, 11); PIN(SC); SB(); \
              if (DOPV) o[3] = MFMA32(vf3, BC8(PP1), o[3]); PC1[0] = cvtpk(SC[8], SC[9]); EX1(SC, 12); PC1[1] = cvtpk(SC[10], SC[11]); PIN(SC); PIN(PC1); SB(); \
              if (DOQK) SN = MFMA32(k0_, qf[4], SN); s1_ += PAIR(SC, 5); PIN2(s1_); EX1(SC, 13); EX1(SC, 14); PIN(SC); SB(); \
              if (DOQK) SN = MFMA32(k1_, qf[5], SN); EX1(SC, 15); PC1[2] = cvtpk(SC[12], SC[13]); PIN(SC); PIN(PC1); SB(); \
            MID; SB(); \
              if (DOQK) SN = MFMA32(k2_, qf[6], SN); s0_ += PAIR(SC, 6); s1_ += PAIR(SC, 7); PIN2(s0_); PIN2(s1_); PC1[3] = cvtpk(SC[14], SC[15]); PIN(PC1); SB(); \
              if (DOQK) SN = MFMA32(k3_, qf[7], SN); lsum += s0_; lsum += s1_; PIN2(lsum); SB(); \
        } while (0)
#define VPRE(a0, a1, vp) do { vf1 = VRDX(a0, a1, vp, 0, 1); vf0 = VRDX(a0, a1, vp, 0, 0); vf3 = VRDX(a0, a1, vp, 0, 3); vf2 = VRDX(a0, a1, vp, 0, 2); } while (0)
#define ABAR(Tn) do { LBAR(); } while (0)
        #pragma unroll 1
        for (int r = 0; r < 2; ++r) {
            {
                BASES();
#pragma unroll
                for (int ks = 0; ks < 8; ++ks) sA = MFMA32(KRD(0, ks), qf[ks], ks ? sA : (f32x16{}));
                float rm; { float a_ = MX3(sA[0], sA[1], sA[2]), b_ = MX3(sA[3], sA[4], sA[5]); a_ = MX3(a_, sA[6], sA[7]); b_ = MX3(b_, sA[8], sA[9]); a_ = MX3(a_, sA[10], sA[11]); b_ = MX3(b_, sA[12], sA[13]);
                    a_ = MX3(a_, sA[14], sA[15]); rm = swap32max(__builtin_fmaxf(a_, b_)); }
#pragma unroll
                for (int i = 0; i < 16; ++i) { sA[i] -= rm; negm[i] = -rm; }
                pend = false;
                SLOTM(sA, sB, pB0, pB1, pA0, pA1, 8192, 0, true, false, { if (r == 0) asm volatile("s_waitcnt vmcnt(0)" ::: "memory");
                    ABAR(16 * r + 1); VPRE(n0p, n1p, 0); });
                ADVANCE();
            }
#pragma unroll 1
            for (int t = 1; t < 16; ++t) {
                BASES();
                SLOTM(sB, sA, pA0, pA1, pB0, pB1, 0, 0, true, true, { VPRE(v0p, v1p, 8192); });
                SLOTM(sA, sB, pB0, pB1, pA0, pA1, 8192, 8192, true, true, { ABAR(16 * r + t + 1); VPRE(n0p, n1p, 0); });
                ADVANCE();
            }
            {
                BASES();
                SLOTM(sB, sA, pA0, pA1, pB0, pB1, 0, 0, false, true, { VPRE(v0p, v1p, 8192); });
                APPLY_PEND();
                o[0] = MFMA32(vf0, BC8(pB0), o[0]); o[1] = MFMA32(vf1, BC8(pB0), o[1]); vf1 = VRD(8192, 1, 1); vf0 = VRD(8192, 1, 0);
                o[2] = MFMA32(vf2, BC8(pB0), o[2]); o[3] = MFMA32(vf3, BC8(pB0), o[3]); vf3 = VRD(8192, 1, 3); vf2 = VRD(8192, 1, 2);
                o[0] = MFMA32(vf0, BC8(pB1), o[0]); o[1] = MFMA32(vf1, BC8(pB1), o[1]); o[2] = MFMA32(vf2, BC8(pB1), o[2]); o[3] = MFMA32(vf3, BC8(pB1), o[3]);
                if (r == 0) {
                    ETAG();
                    unsigned qa = lds0 + EX + 4096u * qb + lo16; asm volatile("" : "+v"(qa));
#pragma unroll
                    for (int s = 0; s < 8; ++s) qf[s] = ldsrd128((lds_cptr)(uintptr_t)qa + 16384 * (s >> 2) + 1024 * (s & 3));
                }
                const float inv = 1.0f / swap32sum(lsum[0] + lsum[1]);
                int ln = lane; asm volatile("" : "+v"(ln));
                const unsigned ovo = (unsigned)((ln >> 4) & 1) * 4096u + (unsigned)(ln & 15) * 16u + (unsigned)(ln >> 5) * 256u
                                   + (unsigned)((((g * 32 + h0 + r) * 8 + 2 * qb) * 4)) * 1024u;
                unsigned lo_ = lds0 + EX + 4096u * qb + (unsigned)((ln >> 4) & 1) * 16384u + (unsigned)(ln & 15) * 16u + (unsigned)(ln >> 5) * 256u; asm volatile("" : "+v"(lo_));
#pragma unroll
                for (int db = 0; db < 4; ++db)
#pragma unroll
                    for (int sg = 0; sg < 2; ++sg) { const bf16x8 pk_ = pack8s(o[db], sg, inv);
                        if (r == 0) *(LAS bf16x8*)(uintptr_t)(lo_ + 1024u * db + 512u * sg) = pk_; else { st16_sc1(BCU(pk_), ovo + 1024u * db + 512u * sg, srdO); pk2[2 * db + sg] = pk_; } }
#pragma unroll
                for (int i = 0; i < 4; ++i) o[i] = f32x16{};
                lsum = f32x2{0.f, 0.f};
            }
        }
                LBAR();
#pragma unroll
        for (int i = 0; i < 2; ++i)
#pragma unroll
            for (int c = 0; c < 9; ++c) oacc[i][c] = f32x4{};
    }
    {
        const int mg = w & 3, kq = w >> 2;
        int ln = lane; asm volatile("" : "+v"(ln));
        const unsigned ab0 = lds0 + (unsigned)(16 * ((ln >> 4) >> 1) + 4 * ((ln >> 4) & 1) + ((ln & 15) >> 2)) * 288u + 8u * (ln & 3);
#define TCHUNK(base, F) do { unsigned a0a = (base) + ab0; asm volatile("" : "+v"(a0a)); const lds_cptr kb_ = (lds_cptr)(uintptr_t)a0a; bf16x8 af_[18]; \
            _Pragma("unroll") for (int ks = 0; ks < 2; ++ks) _Pragma("unroll") for (int c = 0; c < 9; ++c) af_[9 * ks + c] = cat8(vtr(kb_ + 9216 * ks + 32 * c), vtr(kb_ + 9216 * ks + 2304 + 32 * c)); \
            SB(); \
            _Pragma("unroll") for (int ks = 0; ks < 2; ++ks) _Pragma("unroll") for (int c = 0; c < 9; ++c) { \
                oacc[0][c] = MFMA16(af_[9 * ks + c], (F)[2 * ks], oacc[0][c]); oacc[1][c] = MFMA16(af_[9 * ks + c], (F)[2 * ks + 1], oacc[1][c]); } \
            SB(); } while (0)
        if (kq == 0) {
            unsigned sw_ = lds0 + 73728u + 4096u * (unsigned)mg + (unsigned)((ln >> 4) & 1) * 2048u + (unsigned)(ln & 15) * 16u + (unsigned)(ln >> 5) * 256u;
            unsigned sr_ = lds0 + 73728u + 4096u * (unsigned)mg + (unsigned)ln * 16u;
            asm volatile("" : "+v"(sw_), "+v"(sr_));
#pragma unroll
            for (int hf = 0; hf < 2; ++hf) {
#pragma unroll
                for (int d = 0; d < 2; ++d)
#pragma unroll
                    for (int sg = 0; sg < 2; ++sg) *(LAS bf16x8*)(uintptr_t)(sw_ + 1024u * d + 512u * sg) = pk2[4 * hf + 2 * d + sg];
                asm volatile("" ::: "memory");
                bf16x8 f_[4];
#pragma unroll
                for (int d = 0; d < 2; ++d)
#pragma unroll
                    for (int m = 0; m < 2; ++m) f_[2 * d + m] = ldsrd128((lds_cptr)(uintptr_t)(sr_ + 2048u * m + 1024u * d));
                asm volatile("" ::: "memory");
                if (hf == 0) TCHUNK(WS1, f_); else TCHUNK(WS2, f_);
                if (hf == 0) {
                    asm volatile("s_waitcnt vmcnt(0)" ::: "memory");
                    if (ln == 0) st4_sc1(tag, WS_FO + (unsigned)((((g * 4 + hq) * 2 + 1) * 4 + mg) * 4 + c4) * 4u, srdF);
                }
            }
        }
        wait_tag(WS_FO + (unsigned)((((g * 4 + hq) * 2 + 1) * 4 + mg) * 4 + ((c4 + 1 + kq + (lane & 1)) & 3)) * 4u, srdF, tag);
                bf16x8 of[12];
#pragma unroll
        for (int i = 0; i < 6; ++i) {
            const int j2 = 4 + 6 * kq + i;
            const int hd = 8 * hq + 2 * ((c4 + (j2 >> 2)) & 3) + 1;
            const unsigned so = (unsigned)((((g * 32 + hd) * 8 + 2 * mg) * 4) + (j2 & 3)) * 1024u;
            LD16_SC1(of[2 * i], lo16, srdO, RFL(so)); LD16_SC1(of[2 * i + 1], lo16, srdO, RFL(so + 4096u));
        }
        asm volatile("s_waitcnt vmcnt(0)\n\ts_barrier" ::: "memory"); SB();
        asm volatile("" : "+v"(of[0]), "+v"(of[1]), "+v"(of[2]), "+v"(of[3]), "+v"(of[4]), "+v"(of[5]), "+v"(of[6]), "+v"(of[7]) :: "memory");
        asm volatile("" : "+v"(of[8]), "+v"(of[9]), "+v"(of[10]), "+v"(of[11]) :: "memory");
                TCHUNK(kq ? WCH : WS0, of); TCHUNK(kq ? 2u * WCH : WS3, of + 4); TCHUNK(kq ? 3u * WCH : 0u, of + 8);
        LBAR();
        const unsigned xw = (unsigned)w * 9216u + lo16, xr = (unsigned)(w ^ 4) * 9216u + lo16;
#pragma unroll
        for (int c = 0; c < 9; ++c) *(LAS f32x4*)(LW + xw + 1024 * c) = kq ? oacc[0][c] : oacc[1][c];
        LBAR();
        f32x4 fin[9];
#pragma unroll
        for (int c = 0; c < 9; ++c) { const f32x4 t4 = *(const LAS f32x4*)(LW + xr + 1024 * c); fin[c] = (kq ? oacc[1][c] : oacc[0][c]) + t4; }
        const int mb = 2 * mg + kq;
        const unsigned lo8 = (unsigned)lane * 8u;
                if (mg != hq) {
            const unsigned sso = (unsigned)((((g * 4 + c4) * 8 + mb) * 4 + hq) * 9) * 512u + lo8;
#pragma unroll
            for (int c = 0; c < 9; ++c) { const u32x2 v2 = {cvtpk(fin[c][0], fin[c][1]), cvtpk(fin[c][2], fin[c][3])}; st8_sc1(v2, sso + 512u * c, srdS); }
            asm volatile("s_waitcnt vmcnt(0)" ::: "memory");
            if (lane == 0) st4_sc1(tag, WS_FS + (unsigned)(((g * 4 + c4) * 8 + mb) * 4 + hq) * 4u, srdF);
        } else {
            const int ot = (hq + 1 + (lane % 3)) & 3;
            wait_tag(WS_FS + (unsigned)(((g * 4 + c4) * 8 + mb) * 4 + ot) * 4u, srdF, tag);
            if (hq == 0 && c4 == 0 && kq == 0 && lane == 0) st4_sc1(tag, WS_E + 64u * (unsigned)g, srdF);
            u32x2 sv[27];
#pragma unroll
            for (int m = 0; m < 3; ++m)
#pragma unroll
                for (int c = 0; c < 9; ++c) LD8_SC1(sv[9 * m + c], lo8, srdS, RFL((unsigned)((((g * 4 + c4) * 8 + mb) * 4 + ((hq + 1 + m) & 3)) * 9 + c) * 512u));
            asm volatile("s_waitcnt vmcnt(0)" : "+v"(sv[0]), "+v"(sv[1]), "+v"(sv[2]), "+v"(sv[3]), "+v"(sv[4]), "+v"(sv[5]), "+v"(sv[6]), "+v"(sv[7]), "+v"(sv[8]) :: "memory");
            asm volatile("" : "+v"(sv[9]), "+v"(sv[10]), "+v"(sv[11]), "+v"(sv[12]), "+v"(sv[13]), "+v"(sv[14]), "+v"(sv[15]), "+v"(sv[16]), "+v"(sv[17]) :: "memory");
            asm volatile("" : "+v"(sv[18]), "+v"(sv[19]), "+v"(sv[20]), "+v"(sv[21]), "+v"(sv[22]), "+v"(sv[23]), "+v"(sv[24]), "+v"(sv[25]), "+v"(sv[26]) :: "memory");
#pragma unroll
            for (int c = 0; c < 9; ++c) {
                f32x4 a = fin[c];
#pragma unroll
                for (int m = 0; m < 3; ++m) { const u32x2 v = sv[9 * m + c];
                    a[0] += __uint_as_float(v[0] << 16); a[1] += __uint_as_float(v[0] & 0xFFFF0000u); a[2] += __uint_as_float(v[1] << 16); a[3] += __uint_as_float(v[1] & 0xFFFF0000u); }
                const u32x2 ov = {cvtpk(a[0], a[1]), cvtpk(a[2], a[3])};
                *(u32x2*)(out + (size_t)(b * 128 + 16 * mb + (lane & 15)) * 1152 + 576 * p + 144 * c4 + 16 * c + 4 * g4) = ov;
            }
        }
    }
    }

extern "C" void kernel_launch(void* const* d_in, const int* in_sizes, int n_in, void* d_out, int out_size, void* d_ws, size_t ws_size, hipStream_t stream) {
    (void)hipFuncSetAttribute((const void*)fused_kernel, hipFuncAttributeMaxDynamicSharedMemorySize, LDS_BYTES);
    hipLaunchKernelGGL(fused_kernel, dim3(256), dim3(512), LDS_BYTES, stream,
                       (const char*)d_in[0], (const char*)d_in[1], (const char*)d_in[2], (const char*)d_in[3], (const char*)d_in[4],
                       (const char*)d_in[5], (const char*)d_in[6], (const char*)d_in[7], (const char*)d_in[8], (const char*)d_in[9],
                       (char*)d_ws, (unsigned short*)d_out);
}
```

```cpp
#include <hip/hip_runtime.h>
#include <hip/hip_bf16.h>
#include <cstdint>

typedef short bf16x8 __attribute__((ext_vector_type(8)));
typedef short s16x4 __attribute__((ext_vector_type(4)));
typedef float f32x16 __attribute__((ext_vector_type(16)));
typedef float f32x4 __attribute__((ext_vector_type(4)));
typedef float f32x2 __attribute__((ext_vector_type(2)));
typedef __bf16 bf16x2_t __attribute__((ext_vector_type(2)));
typedef unsigned u32x4 __attribute__((ext_vector_type(4)));
typedef unsigned u32x2 __attribute__((ext_vector_type(2)));
#define LAS __attribute__((address_space(3)))
#define DI __device__ __forceinline__
typedef LAS const char* lds_cptr;
typedef LAS char* lds_ptr;

constexpr int KVBYTES = 8 * 32 * 1024 * 256;
constexpr unsigned SLOT = 16384;
constexpr int LDS_BYTES = 163840;
constexpr float CSC = 0.08838834764831845f * 1.4426950408889634f;
constexpr float THR = 8.0f;
constexpr unsigned EX = 6 * SLOT, WR = 8 * SLOT;
constexpr unsigned WCH = 18432u, WS0 = 108544u, WS1 = WS0 + WCH, WS2 = WS0 + 2 * WCH, WS3 = WS0 - WCH;
constexpr unsigned WS_E = 0, WS_FO = 4096, WS_FS = 16384;
constexpr size_t WS_O = 65536, WS_O_BYTES = (size_t)2048 * 4096 * 2;
constexpr size_t WS_SL = WS_O + WS_O_BYTES, WS_SL_BYTES = (size_t)16 * 4 * 8 * 4 * 9 * 512;
constexpr unsigned SPIN_CAP = 1u << 20;

#define MFMA32(a, b, c) __builtin_amdgcn_mfma_f32_32x32x16_bf16((a), (b), (c), 0, 0, 0)
#define MFMA16(a, b, c) __builtin_amdgcn_mfma_f32_16x16x32_bf16((a), (b), (c), 0, 0, 0)
#define RFL(x) __builtin_amdgcn_readfirstlane(x)
#define WAITBAR(N) asm volatile("s_waitcnt vmcnt(" #N ") lgkmcnt(0)\n\ts_barrier" ::: "memory")
template <int N> __device__ __forceinline__ void waitbar() { asm volatile("s_waitcnt vmcnt(%0) lgkmcnt(0)\n\ts_barrier" :: "i"(N) : "memory"); }
template <int N> __device__ __forceinline__ void waitv() { asm volatile("s_waitcnt vmcnt(%0)" :: "i"(N) : "memory"); }
#define WB17 WAITBAR(17)
#define WWIN 3
#define XWIN 4
#define LBAR() asm volatile("s_waitcnt lgkmcnt(0)\n\ts_barrier" ::: "memory")
#define SB() __builtin_amdgcn_sched_barrier(0)
#define PIN(x) asm volatile("" : "+v"(x))
#define PIN2(x) asm volatile("" : "+v"(x))

DI unsigned cvtpk(float lo, float hi) { f32x2 v = {lo, hi}; bf16x2_t b = __builtin_convertvector(v, bf16x2_t); return __builtin_bit_cast(unsigned, b); }
DI s16x4 vtr(lds_cptr p) { return __builtin_bit_cast(s16x4, __builtin_amdgcn_ds_read_tr16_b64_v4i16((LAS s16x4*)p)); }
DI bf16x8 cat8(s16x4 a, s16x4 b) { return __builtin_shufflevector(a, b, 0, 1, 2, 3, 4, 5, 6, 7); }
DI bf16x8 ldsrd128(lds_cptr p) { return *(const LAS bf16x8*)p; }
DI float swap32max(float m) { auto rr = __builtin_amdgcn_permlane32_swap(__float_as_uint(m), __float_as_uint(m), false, false); return __builtin_fmaxf(__uint_as_float(rr[0]), __uint_as_float(rr[1])); }
DI float swap32sum(float m) { auto rr = __builtin_amdgcn_permlane32_swap(__float_as_uint(m), __float_as_uint(m), false, false); return __uint_as_float(rr[0]) + __uint_as_float(rr[1]); }
DI bf16x8 pack8s(const f32x16& a, int sg, float sc) {
    u32x4 wv = {cvtpk(a[8 * sg + 0] * sc, a[8 * sg + 1] * sc), cvtpk(a[8 * sg + 2] * sc, a[8 * sg + 3] * sc), cvtpk(a[8 * sg + 4] * sc, a[8 * sg + 5] * sc), cvtpk(a[8 * sg + 6] * sc, a[8 * sg + 7] * sc)};
    return __builtin_bit_cast(bf16x8, wv);
}
#define BC8(x) __builtin_bit_cast(bf16x8, x)
#define BCU(x) __builtin_bit_cast(u32x4, x)

DI void dma16_nt(unsigned ldsdst, unsigned voff, __amdgpu_buffer_rsrc_t srd, unsigned soff) {
    asm volatile("s_mov_b32 m0, %0\n\ts_nop 0\n\tbuffer_load_dwordx4 %1, %2, %3 offen nt lds" :: "s"(ldsdst), "v"(voff), "s"(srd), "s"(soff) : "memory");
}
DI void dma16(unsigned ldsdst, unsigned voff, __amdgpu_buffer_rsrc_t srd, unsigned soff) {
    asm volatile("s_mov_b32 m0, %0\n\ts_nop 0\n\tbuffer_load_dwordx4 %1, %2, %3 offen lds" :: "s"(ldsdst), "v"(voff), "s"(srd), "s"(soff) : "memory");
}
DI void st16_sc1(u32x4 v, unsigned voff, __amdgpu_buffer_rsrc_t srd) { __builtin_amdgcn_raw_buffer_store_b128(v, srd, (int)voff, 0, 16); }
DI void st8_sc1(u32x2 v, unsigned voff, __amdgpu_buffer_rsrc_t srd) { __builtin_amdgcn_raw_buffer_store_b64(v, srd, (int)voff, 0, 16); }
DI void st4_sc1(unsigned v, unsigned voff, __amdgpu_buffer_rsrc_t srd) { __builtin_amdgcn_raw_buffer_store_b32(v, srd, (int)voff, 0, 16); }
#define LD16_SC1(dst, voff, srd, soff) asm volatile("buffer_load_dwordx4 %0, %1, %2, %3 offen sc1" : "=v"(dst) : "v"(voff), "s"(srd), "s"(soff) : "memory")
#define LD8_SC1(dst, voff, srd, soff) asm volatile("buffer_load_dwordx2 %0, %1, %2, %3 offen sc1" : "=v"(dst) : "v"(voff), "s"(srd), "s"(soff) : "memory")
#define LD4_SC1(dst, voff, srd) asm volatile("buffer_load_dword %0, %1, %2, 0 offen sc1" : "=v"(dst) : "v"(voff), "s"(srd) : "memory")
#define LD16(dst, voff, srd, soff) asm volatile("buffer_load_dwordx4 %0, %1, %2, %3 offen" : "=v"(dst) : "v"(voff), "s"(srd), "s"(soff) : "memory")
DI void wait_tag(unsigned voff, __amdgpu_buffer_rsrc_t srd, unsigned tag) {
    for (unsigned sp = 0; sp < SPIN_CAP; ++sp) {
        unsigned f;
        asm volatile("buffer_load_dword %0, %1, %2, 0 offen sc1\n\ts_waitcnt vmcnt(0)" : "=v"(f) : "v"(voff), "s"(srd) : "memory");
        if (__all(f == tag)) break;
        __builtin_amdgcn_s_sleep(1);
    }
}

__global__ __launch_bounds__(512, 2) void fused_kernel(const char* __restrict__ xA, const char* __restrict__ kA, const char* __restrict__ vA, const char* __restrict__ wqA, const char* __restrict__ woA,
                                                      const char* __restrict__ xB, const char* __restrict__ kB, const char* __restrict__ vB, const char* __restrict__ wqB, const char* __restrict__ woB,
                                                      char* __restrict__ ws, unsigned short* __restrict__ out) {
    extern __shared__ __attribute__((aligned(16))) char lds[];
    const int tid = threadIdx.x, lane = tid & 63;
    const int w = RFL(tid >> 6);
    const int r32 = lane & 31, hi = lane >> 5;
    const int wg = blockIdx.x, xcd = wg & 7, idx = wg >> 3;
    const int p = xcd & 1, hq = xcd >> 1, c4 = idx & 3, b = idx >> 2;
    const int g = p * 8 + b;
    const int h0 = 8 * hq + 2 * c4;
    const unsigned lds0 = (unsigned)(uintptr_t)lds;
    const lds_cptr L = (lds_cptr)lds;
    const lds_ptr LW = (lds_ptr)lds;
        const __amdgpu_buffer_rsrc_t srdX = __builtin_amdgcn_make_buffer_rsrc((void*)(p ? xB : xA), (short)0, 8 * 128 * 576 * 2, 0x00020000);
    const __amdgpu_buffer_rsrc_t srdW = __builtin_amdgcn_make_buffer_rsrc((void*)(p ? wqB : wqA), (short)0, 576 * 4096 * 2, 0x00020000);
    const __amdgpu_buffer_rsrc_t srdK = __builtin_amdgcn_make_buffer_rsrc((void*)(p ? kB : kA), (short)0, KVBYTES, 0x00020000);
    const __amdgpu_buffer_rsrc_t srdV = __builtin_amdgcn_make_buffer_rsrc((void*)(p ? vB : vA), (short)0, KVBYTES, 0x00020000);
    const __amdgpu_buffer_rsrc_t srdWo = __builtin_amdgcn_make_buffer_rsrc((void*)(p ? woB : woA), (short)0, 4096 * 576 * 2, 0x00020000);
    const __amdgpu_buffer_rsrc_t srdO = __builtin_amdgcn_make_buffer_rsrc((void*)(ws + WS_O), (short)0, (int)WS_O_BYTES, 0x00020000);
    const __amdgpu_buffer_rsrc_t srdS = __builtin_amdgcn_make_buffer_rsrc((void*)(ws + WS_SL), (short)0, (int)WS_SL_BYTES, 0x00020000);
    const __amdgpu_buffer_rsrc_t srdF = __builtin_amdgcn_make_buffer_rsrc((void*)ws, (short)0, 65536, 0x00020000);

    const bool isB = w >= 4;
    const int bw = w & 3;
    const int r7 = (lane >> 2) & 7;
    const int blk = (lane >> 4) & 1, q4 = (lane & 15) >> 2, p4 = lane & 3, g4 = lane >> 4;
    const unsigned lo16 = (unsigned)lane * 16u;
    unsigned ev;
#define ELOAD() do { const unsigned eo_ = WS_E + 64u * (unsigned)g; asm volatile("buffer_load_dword %0, %1, %2, 0 offen sc1" : "=v"(ev) : "v"(eo_), "s"(srdF) : "memory"); } while (0)
#define ETAG() do { PIN(ev); tag = (unsigned)RFL(ev) * 0x9E3779B1u + 0x7F4A7C15u; } while (0)
    const unsigned chs0 = 16u * (4u * (unsigned)hi + (unsigned)((lane & 3) ^ ((2 * (2 * bw) + (r7 >> 2)) & 3)));
    const unsigned chs1 = 16u * (4u * (unsigned)hi + (unsigned)((lane & 3) ^ ((2 * (2 * bw + 1) + (r7 >> 2)) & 3)));
    const unsigned srcKV0 = (unsigned)(16 * bw + r7) * 256u + chs0, srcKV1 = (unsigned)(16 * bw + 8 + r7) * 256u + chs1;
    const unsigned srcW0 = (unsigned)(16 * bw + r7) * 8192u + chs0, srcW1 = (unsigned)(16 * bw + 8 + r7) * 8192u + chs1;
    auto kv_off = [&](int T) -> unsigned { return (unsigned)((b * 32 + h0) * 1024 + 64 * T) * 256u; };
    auto kslot = [&](int T) -> unsigned { return (unsigned)(T % 3) * SLOT; };
    auto vslot = [&](int T) -> unsigned { return (unsigned)(3 + T % 3) * SLOT; };
    auto dma_tile = [&](__amdgpu_buffer_rsrc_t srd, int T, unsigned slot) { const unsigned so = kv_off(T), d = lds0 + 4096u * (unsigned)bw + slot;
        dma16_nt(RFL(d), srcKV0, srd, RFL(so)); dma16_nt(RFL(d + 1024), srcKV0, srd, RFL(so + 128));
        dma16_nt(RFL(d + 2048), srcKV1, srd, RFL(so)); dma16_nt(RFL(d + 3072), srcKV1, srd, RFL(so + 128)); };
    auto dma_k = [&](int T) { dma_tile(srdK, T, kslot(T)); };
    auto dma_v = [&](int T) { dma_tile(srdV, T, vslot(T)); };
    const int rot = (b + 2 * c4) % 9;
    auto dma_wjob = [&](int head, int n, unsigned dst) {
        const int ps = n / 9, kc0 = n - 9 * ps + rot, kc = kc0 >= 9 ? kc0 - 9 : kc0;
        const unsigned sw = (unsigned)(64 * kc) * 8192u + (unsigned)head * 256u + 128u * (unsigned)ps, d = lds0 + dst + 2048u * (unsigned)bw;
        dma16(RFL(d), srcW0, srdW, RFL(sw)); dma16(RFL(d + 1024), srcW1, srdW, RFL(sw));
    };
    auto dma_wdj = [&](int head, int gj, unsigned dst) { dma_wjob(head, 2 * gj, dst); dma_wjob(head, 2 * gj + 1, dst + 8192u); };
    unsigned tag;

    bf16x8 pk2[8];
    f32x4 oacc[2][9];
    if (isB) {
        bf16x8 xf[36];
                const unsigned vx = (unsigned)(32 * bw + (lane >> 3)) * 1152u + 16u * (lane & 7);
        const unsigned sx = (unsigned)(b * 128) * 1152u;
#define XLOAD(c) do { const int kk_ = (c) + rot; const unsigned so_ = sx + 128u * (unsigned)(kk_ >= 9 ? kk_ - 9 : kk_); \
            LD16(xf[4 * (c)], vx, srdX, RFL(so_)); LD16(xf[4 * (c) + 1], vx, srdX, RFL(so_ + 9216u)); LD16(xf[4 * (c) + 2], vx, srdX, RFL(so_ + 18432u)); LD16(xf[4 * (c) + 3], vx, srdX, RFL(so_ + 27648u)); } while (0)
        XLOAD(0); XLOAD(1); XLOAD(2); XLOAD(3);
        if (XWIN > 4) XLOAD(4); if (XWIN > 5) XLOAD(5); if (XWIN > 6) XLOAD(6); if (XWIN > 7) XLOAD(7); if (XWIN > 8) XLOAD(8);
        const unsigned xwe = lds0 + EX + 4096u * bw + (unsigned)(lane >> 3) * 128u + 16u * (unsigned)((lane & 7) ^ ((lane >> 4) & 7));
        const unsigned xwo = lds0 + EX + 4096u * bw + (unsigned)(8 + (lane >> 3)) * 128u + 16u * (unsigned)((lane & 7) ^ ((4 + (lane >> 4)) & 7));
        const unsigned xrd = lds0 + EX + 4096u * bw + (unsigned)r32 * 128u + 16u * (unsigned)(hi ^ ((r32 >> 1) & 7));
        const unsigned wb0 = lds0 + 1024u * hi + 64u * (0 + q4) + 16u * ((2 * blk + (p4 & 1)) ^ (2 * hi + 0)) + 8u * (p4 >> 1);
        const unsigned wb1 = lds0 + 1024u * hi + 64u * (4 + q4) + 16u * ((2 * blk + (p4 & 1)) ^ (2 * hi + 1)) + 8u * (p4 >> 1);
        const unsigned qex = EX + 4096u * bw + lo16;
        unsigned tch = 0;
#define TOUCH(vo, srd, so) asm volatile("buffer_load_dword %0, %1, %2, %3 offen" : "+v"(tch) : "v"(vo), "s"(srd), "s"(so) : "memory")
#define TOUCH_WO(r) do { int l_ = lane < 36 ? lane : 35; asm volatile("" : "+v"(l_)); const unsigned so_ = (unsigned)(128 * (h0 + (r)) + 16 * b + 4 * bw) * 1152u; \
            TOUCH((unsigned)l_ * 128u, srdWo, RFL(so_)); } while (0)
        f32x16 acc[2];
        bf16x8 ar[8];
#define XCONV(c) do { asm volatile("s_waitcnt vmcnt(%4)" : "+v"(xf[4 * (c)]), "+v"(xf[4 * (c) + 1]), "+v"(xf[4 * (c) + 2]), "+v"(xf[4 * (c) + 3]) : "i"(4 * ((((c) - 1 + XWIN < 8) ? (c) - 1 + XWIN : 8) - (c))) : "memory"); \
            *(LAS bf16x8*)(uintptr_t)(xwe) = xf[4 * (c)]; *(LAS bf16x8*)(uintptr_t)(xwo) = xf[4 * (c) + 1]; *(LAS bf16x8*)(uintptr_t)(xwe + 2048) = xf[4 * (c) + 2]; *(LAS bf16x8*)(uintptr_t)(xwo + 2048) = xf[4 * (c) + 3]; \
            asm volatile("" ::: "memory"); \
            _Pragma("unroll") for (int ks = 0; ks < 4; ++ks) xf[4 * (c) + ks] = ldsrd128((lds_cptr)(uintptr_t)(xrd ^ (32u * ks))); \
            asm volatile("" ::: "memory"); \
            if ((c) + XWIN <= 8) XLOAD(((c) + XWIN <= 8) ? (c) + XWIN : 0); } while (0)
#define WFR(wp0, wp1, m) cat8(vtr((wp0) + 8192 * ((m) >> 3) + 2048 * (((m) >> 1) & 3) + 512 * ((m) & 1)), vtr((wp1) + 8192 * ((m) >> 3) + 2048 * (((m) >> 1) & 3) + 512 * ((m) & 1)))
#define WPTR(p0, p1, base) unsigned p0##a = wb0 + (base), p1##a = wb1 + (base); asm volatile("" : "+v"(p0##a), "+v"(p1##a)); const lds_cptr p0 = (lds_cptr)(uintptr_t)p0##a, p1 = (lds_cptr)(uintptr_t)p1##a
#define QMF(gj, m) do { constexpr int n_ = 2 * (gj) + ((m) >> 3), kc_ = n_ % 9, ks_ = ((m) >> 1) & 3, dl_ = (m) & 1; \
            if (kc_ == 0 && ks_ == 0) acc[dl_] = MFMA32(ar[(m) & 7], xf[4 * kc_ + ks_], (f32x16{})); else acc[dl_] = MFMA32(ar[(m) & 7], xf[4 * kc_ + ks_], acc[dl_]); \
            PIN(acc[dl_]); \
            if (kc_ == 8 && ((m) & 7) == 7) { _Pragma("unroll") for (int dl = 0; dl < 2; ++dl) _Pragma("unroll") for (int sg = 0; sg < 2; ++sg) \
                *(LAS bf16x8*)(LW + qex + 16384u * (n_ / 9) + (2 * dl + sg) * 1024) = pack8s(acc[dl], sg, CSC); } } while (0)
#define QSTREAM(gj, base, MID, HASNEXT, nbase) do { \
            { WPTR(c0, c1, base); \
              QMF(gj, 0); ar[6] = WFR(c0, c1, 6); SB(); QMF(gj, 1); ar[7] = WFR(c0, c1, 7); SB(); QMF(gj, 2); ar[0] = WFR(c0, c1, 8); SB(); QMF(gj, 3); ar[1] = WFR(c0, c1, 9); SB(); \
              QMF(gj, 4); ar[2] = WFR(c0, c1, 10); SB(); QMF(gj, 5); ar[3] = WFR(c0, c1, 11); SB(); QMF(gj, 6); ar[4] = WFR(c0, c1, 12); SB(); QMF(gj, 7); ar[5] = WFR(c0, c1, 13); SB(); \
              QMF(gj, 8); ar[6] = WFR(c0, c1, 14); SB(); QMF(gj, 9); ar[7] = WFR(c0, c1, 15); SB(); QMF(gj, 10); SB(); QMF(gj, 11); SB(); } \
            MID; SB(); \
            if (HASNEXT) { WPTR(n0, n1, nbase); \
              QMF(gj, 12); ar[0] = WFR(n0, n1, 0); ar[1] = WFR(n0, n1, 1); SB(); QMF(gj, 13); ar[2] = WFR(n0, n1, 2); ar[3] = WFR(n0, n1, 3); SB(); \
              QMF(gj, 14); SB(); QMF(gj, 15); ar[4] = WFR(n0, n1, 4); ar[5] = WFR(n0, n1, 5); SB(); } \
            else { QMF(gj, 12); SB(); QMF(gj, 13); SB(); QMF(gj, 14); SB(); QMF(gj, 15); SB(); } \
        } while (0)
#define QDJ(T, gj, base) do { WPTR(c0, c1, base); \
            dma_k((T) + 2); \
            _Pragma("unroll") for (int m = 0; m < 8; ++m) ar[m] = WFR(c0, c1, m); SB(); \
            QMF(gj, 0); QMF(gj, 1); QMF(gj, 2); QMF(gj, 3); QMF(gj, 4); QMF(gj, 5); QMF(gj, 6); QMF(gj, 7); SB(); \
            dma_v((T) + 1); \
            _Pragma("unroll") for (int m = 8; m < 16; ++m) ar[m & 7] = WFR(c0, c1, m); SB(); \
            QMF(gj, 8); QMF(gj, 9); QMF(gj, 10); QMF(gj, 11); QMF(gj, 12); QMF(gj, 13); QMF(gj, 14); QMF(gj, 15); SB(); } while (0)
                XCONV(0); XCONV(1);
        LBAR();         { WPTR(c0, c1, 0u); ar[0] = WFR(c0, c1, 0); ar[1] = WFR(c0, c1, 1); ar[2] = WFR(c0, c1, 2); ar[3] = WFR(c0, c1, 3); ar[4] = WFR(c0, c1, 4); ar[5] = WFR(c0, c1, 5); }
        QSTREAM(0, 0u, LBAR(), true, SLOT);     XCONV(2); XCONV(3);         QSTREAM(1, SLOT, LBAR(), true, 3 * SLOT);          XCONV(4); XCONV(5);
        QSTREAM(2, 3 * SLOT, LBAR(), true, 2 * SLOT);      XCONV(6); XCONV(7);
        QSTREAM(3, 2 * SLOT, LBAR(), true, 4 * SLOT);      XCONV(8);
        QSTREAM(4, 4 * SLOT, LBAR(), true, 5 * SLOT);         QSTREAM(5, 5 * SLOT, LBAR(), true, EX + SLOT);
        QSTREAM(6, EX + SLOT, LBAR(), true, WR);
        QSTREAM(7, WR, LBAR(), true, WR + SLOT);
        QSTREAM(8, WR + SLOT, {}, false, 0u);
                LBAR();
#define BKV(T) do { if ((T) < 30) dma_k((T) + 2); if ((T) < 31) dma_v((T) + 1); } while (0)
        BKV(0); dma_wdj(h0 + 1, 0, WR); dma_wdj(h0 + 1, 1, WR + SLOT); WAITBAR(16);
        BKV(1); ELOAD(); WAITBAR(9);
        QDJ(2, 0, WR); WAITBAR(8);
        QDJ(3, 1, WR + SLOT); ETAG(); dma_wdj(h0 + 1, 2, WR); WAITBAR(12);
        BKV(4); dma_wdj(h0 + 1, 3, WR + SLOT); WAITBAR(12);
        QDJ(5, 2, WR); WAITBAR(8);
        QDJ(6, 3, WR + SLOT); dma_wdj(h0 + 1, 4, WR); WAITBAR(12);
        BKV(7); dma_wdj(h0 + 1, 5, WR + SLOT); WAITBAR(12);
        QDJ(8, 4, WR); WAITBAR(8);
        QDJ(9, 5, WR + SLOT); dma_wdj(h0 + 1, 6, WR); WAITBAR(12);
        BKV(10); dma_wdj(h0 + 1, 7, WR + SLOT); WAITBAR(12);
        QDJ(11, 6, WR); WAITBAR(8);
        QDJ(12, 7, WR + SLOT); dma_wdj(h0 + 1, 8, WR); WAITBAR(12);
        BKV(13); TOUCH_WO(0); WAITBAR(9);
        QDJ(14, 8, WR); WAITBAR(8);
        BKV(15); WAITBAR(8);
        {
            int ln = lane; asm volatile("" : "+v"(ln));
            unsigned wov[5];
#pragma unroll
            for (int i = 0; i < 5; ++i) {
                const int pc = (i < 4) ? bw + 4 * i : 16 + (bw & 1);
                const int uu = 64 * pc + ln, pos = uu / 18, ch = uu - 18 * pos;
                const int k = (pos & ~12) | ((pos & 4) << 1) | ((pos & 8) >> 1);
                wov[i] = (unsigned)k * 1152u + 16u * ch;
            }
            const unsigned ab0 = lds0 + (unsigned)(16 * ((ln >> 4) >> 1) + 4 * ((ln >> 4) & 1) + ((ln & 15) >> 2)) * 288u + 8u * (ln & 3);
            auto dma_wo = [&](int r, int jj, unsigned dst) {
                const int hd = 8 * hq + 2 * ((c4 + (jj >> 1)) & 3) + r;
                const unsigned so = (unsigned)(128 * hd + 64 * (jj & 1)) * 1152u + 288u * c4;
#pragma unroll
                for (int i = 0; i < 5; ++i) { const int pc = (i < 4) ? bw + 4 * i : 16 + (bw & 1); dma16(RFL(lds0 + dst + 1024u * pc), wov[i], srdWo, RFL(so)); }
            };
#pragma unroll
            for (int i = 0; i < 2; ++i)
#pragma unroll
                for (int c = 0; c < 9; ++c) oacc[i][c] = f32x4{};
            bf16x8 ofx[4], ofy[4];
            unsigned flagv;
            const unsigned fo1 = WS_FO + (unsigned)((((g * 4 + hq) * 2 + 0) * 4 + bw) * 4 + (ln & 3)) * 4u;
#define O1L(OF, jj) do { const int hd_ = 8 * hq + 2 * ((c4 + ((jj) >> 1)) & 3); const unsigned so_ = (unsigned)((((g * 32 + hd_) * 8 + 2 * bw) * 4) + 2 * ((jj) & 1)) * 1024u; \
                LD16_SC1(OF[0], lo16, srdO, RFL(so_)); LD16_SC1(OF[1], lo16, srdO, RFL(so_ + 4096u)); LD16_SC1(OF[2], lo16, srdO, RFL(so_ + 1024u)); LD16_SC1(OF[3], lo16, srdO, RFL(so_ + 5120u)); } while (0)
#define OPHALF(base, OF, ks) do { \
                unsigned a0a = ab0 + (base) + 9216u * (ks); asm volatile("" : "+v"(a0a)); \
                const lds_cptr kb_ = (lds_cptr)(uintptr_t)a0a; bf16x8 af_[9]; \
                _Pragma("unroll") for (int c = 0; c < 9; ++c) af_[c] = cat8(vtr(kb_ + 32 * c), vtr(kb_ + 2304 + 32 * c)); \
                SB(); \
                _Pragma("unroll") for (int c = 0; c < 9; ++c) { oacc[0][c] = MFMA16(af_[c], OF[2 * (ks)], oacc[0][c]); oacc[1][c] = MFMA16(af_[c], OF[2 * (ks) + 1], oacc[1][c]); PIN(oacc[0][c]); PIN(oacc[1][c]); } \
                SB(); \
            } while (0)
#define WSL(jj) ((jj) % 3 == 0 ? WS2 : (jj) % 3 == 1 ? WS0 : WS1)
#define ISSUE1(jj, OF) do { dma_wo(0, jj, WSL(jj)); O1L(OF, jj); } while (0)
#define BKVC(T, jj, OF) do { asm volatile("" : "+v"(OF[0]), "+v"(OF[1]), "+v"(OF[2]), "+v"(OF[3]) :: "memory"); \
                if ((T) < 30) dma_k((T) + 2); OPHALF(WSL(jj), OF, 0); if ((T) < 31) dma_v((T) + 1); OPHALF(WSL(jj), OF, 1); } while (0)
            BKV(16); WAITBAR(8);
            {
                unsigned oa = lds0 + EX + 4096u * bw + lo16; asm volatile("" : "+v"(oa)); const lds_cptr op = (lds_cptr)(uintptr_t)oa;
                ofx[0] = ldsrd128(op); ofx[1] = ldsrd128(op + 16384); ofx[2] = ldsrd128(op + 1024); ofx[3] = ldsrd128(op + 17408);
                ofy[0] = ldsrd128(op + 2048); ofy[1] = ldsrd128(op + 18432); ofy[2] = ldsrd128(op + 3072); ofy[3] = ldsrd128(op + 19456); }
            BKV(17); dma_wo(0, 0, WSL(0));
            {   const unsigned so_ = (unsigned)((((g * 32 + h0) * 8 + 2 * bw) * 4)) * 1024u + lo16;
#pragma unroll
                for (int i = 0; i < 4; ++i) { st16_sc1(BCU(ofx[i]), so_ + 4096u * (i & 1) + 1024u * (i >> 1), srdO); st16_sc1(BCU(ofy[i]), so_ + 4096u * (i & 1) + 1024u * (2 + (i >> 1)), srdO); } }
            WAITBAR(21);
            BKV(18); dma_wo(0, 1, WSL(1)); WAITBAR(13);
            if (ln == 0) st4_sc1(tag, WS_FO + (unsigned)((((g * 4 + hq) * 2 + 0) * 4 + bw) * 4 + c4) * 4u, srdF);
            BKVC(19, 0, ofx); WAITBAR(8);
            BKVC(20, 1, ofy); WAITBAR(8);
            LD4_SC1(flagv, fo1, srdF);
            BKV(21); TOUCH_WO(1); WAITBAR(9);
            BKV(22);
            asm volatile("" : "+v"(flagv) :: "memory");
            if (!__all(flagv == tag)) wait_tag(fo1, srdF, tag);
            ISSUE1(2, ofx); WB17; asm volatile("" :: "v"(tch));
            BKV(23); ISSUE1(3, ofy); WB17;
            BKVC(24, 2, ofx); ISSUE1(4, ofx); WB17;
            BKVC(25, 3, ofy); ISSUE1(5, ofy); WB17;
            BKVC(26, 4, ofx); ISSUE1(6, ofx); WB17;
            BKVC(27, 5, ofy); ISSUE1(7, ofy); WB17;
            BKVC(28, 6, ofx); dma_wo(1, 0, WS1); WAITBAR(13);
            BKVC(29, 7, ofy); dma_wo(1, 1, WS2); WAITBAR(13);
            BKV(30); dma_wo(1, 2, WS0); WAITBAR(9);
            BKV(31); dma_wo(1, 3, WS3); WAITBAR(5);
                                    WAITBAR(0);
            dma_wo(1, 4, 0u); dma_wo(1, 5, WCH); dma_wo(1, 6, 2u * WCH);
            dma_wo(1, 7, 3u * WCH);
        }
    } else {
        {
            constexpr unsigned PS[9] = {0u, SLOT, 3 * SLOT, 2 * SLOT, 4 * SLOT, 5 * SLOT, EX + SLOT, WR, WR + SLOT};
#pragma unroll
            for (int s = 0; s < WWIN; ++s) dma_wdj(h0, s, PS[s]);
            #define PSTEP(s) do { waitbar<4 * (((s) - 1 + WWIN < 8 ? (s) - 1 + WWIN : 8) - (s)) + ((s) == 7 ? 4 : (s) == 8 ? 12 : 0)>(); \
                if ((s) + WWIN <= 8) dma_wdj(h0, ((s) + WWIN <= 8) ? (s) + WWIN : 0, PS[((s) + WWIN <= 8) ? (s) + WWIN : 0]); } while (0)
            PSTEP(0); PSTEP(1); PSTEP(2); PSTEP(3); PSTEP(4); PSTEP(5);
            PSTEP(6); dma_k(0);
            PSTEP(7); dma_k(1); dma_v(0);
            PSTEP(8);
            WAITBAR(8);
            ELOAD();
        }
        const int qb = w;
        bf16x8 qf[8];
        { unsigned qa = lds0 + EX + 4096u * qb + lo16; asm volatile("" : "+v"(qa));
#pragma unroll
          for (int s = 0; s < 8; ++s) qf[s] = ldsrd128((lds_cptr)(uintptr_t)qa + 16384 * (s >> 2) + 1024 * (s & 3)); }
        const unsigned kb0 = 2048u * (r32 >> 3) + 64u * (r32 & 7) + 16u * ((0 + hi) ^ ((r32 >> 2) & 3));
        const unsigned kb1 = 2048u * (r32 >> 3) + 64u * (r32 & 7) + 16u * ((2 + hi) ^ ((r32 >> 2) & 3));
        const unsigned vb0 = 64u * (4 * hi + q4) + 16u * ((2 * blk + (p4 & 1)) ^ (0 + hi)) + 8u * (p4 >> 1);
        const unsigned vb1 = 2048u + 64u * (4 * hi + q4) + 16u * ((2 * blk + (p4 & 1)) ^ (2 + hi)) + 8u * (p4 >> 1);
        f32x16 o[4];
#pragma unroll
        for (int i = 0; i < 4; ++i) o[i] = f32x16{};
        f32x2 lsum = {0.f, 0.f};
        float fpend = 1.f; bool pend = false;
        f32x16 sA = f32x16{}, sB = f32x16{}, negm = f32x16{};
        u32x4 pA0 = {}, pA1 = {}, pB0 = {}, pB1 = {};
        bf16x8 vf0 = {}, vf1 = {}, vf2 = {}, vf3 = {};
        const unsigned kbb0 = lds0 + kb0, kbb1 = lds0 + kb1, vbb0 = lds0 + vb0 + 3 * SLOT, vbb1 = lds0 + vb1 + 3 * SLOT;
        unsigned scur = 0, sprev = 2 * SLOT;
#define KRD(kp, ks) ldsrd128((((ks) & 1) ? k1p : k0p) + (kp) + 512 * ((ks) >> 1))
#define VRDX(a0, a1, vp, sg, db) cat8(vtr((a0) + (vp) + 4096 * (sg) + 512 * (db)), vtr((a1) + (vp) + 4096 * (sg) + 512 * (db)))
#define VRD(vp, sg, db) VRDX(v0p, v1p, vp, sg, db)
#define BASES() asm volatile("" : "+s"(scur), "+s"(sprev)); \
            unsigned k0a = kbb0 + scur, k1a = kbb1 + scur, v0a = vbb0 + sprev, v1a = vbb1 + sprev, n0a = vbb0 + scur, n1a = vbb1 + scur; \
            asm volatile("" : "+v"(k0a), "+v"(k1a), "+v"(v0a), "+v"(v1a), "+v"(n0a), "+v"(n1a)); \
            const lds_cptr k0p = (lds_cptr)(uintptr_t)k0a, k1p = (lds_cptr)(uintptr_t)k1a, v0p = (lds_cptr)(uintptr_t)v0a, v1p = (lds_cptr)(uintptr_t)v1a, n0p = (lds_cptr)(uintptr_t)n0a, n1p = (lds_cptr)(uintptr_t)n1a
#define ADVANCE() do { sprev = scur; scur = (scur == 2 * SLOT) ? 0u : scur + SLOT; } while (0)
#define EX1(S, i) do { S[i] = __builtin_amdgcn_exp2f(S[i]); } while (0)
#define MX3(a, b, c) __builtin_fmaxf(__builtin_fmaxf(a, b), c)
#define AMAX3(d, a, b, c) asm volatile("v_max3_f32 %0, %1, %2, %3" : "=v"(d) : "v"(a), "v"(b), "v"(c))
#define AMAX2(d, a, b) asm volatile("v_max_f32 %0, %1, %2" : "=v"(d) : "v"(a), "v"(b))
#define PAIR(S, i) (f32x2{S[2 * (i)], S[2 * (i) + 1]})
#define RMAX1(S) ma_ = MX3(S[0], S[1], S[2]); mb_ = MX3(S[3], S[4], S[5]); mc_ = MX3(S[6], S[7], S[8]); md_ = MX3(S[9], S[10], S[11]); me_ = MX3(S[12], S[13], S[14]); \
            ma_ = MX3(ma_, mb_, mc_); md_ = MX3(md_, me_, S[15]); ma_ = MX3(ma_, md_, 0.f); PIN(ma_)
#define RMAX2() do { auto rr_ = __builtin_amdgcn_permlane32_swap(__float_as_uint(ma_), __float_as_uint(ma_), false, false); \
            rm_ = MX3(__uint_as_float(rr_[0]), __uint_as_float(rr_[1]), 0.f); PIN(rm_); } while (0)
#define APPLY_PEND() do { if (__builtin_expect(pend, 0)) { _Pragma("unroll") for (int d = 0; d < 4; ++d) _Pragma("unroll") for (int i = 0; i < 16; ++i) o[d][i] *= fpend; pend = false; } } while (0)
#define DECIDE(SN, PP1, DOPV) do { \
            if (__builtin_expect(__any(rm_ > THR), 0)) { const float dl_ = rm_; \
                _Pragma("unroll") for (int i = 0; i < 16; ++i) { SN[i] -= dl_; negm[i] -= dl_; } \
                fpend = __builtin_amdgcn_exp2f(-dl_); lsum *= fpend; pend = true; } \
        } while (0)
#define SLOTM(SC, SN, PP0, PP1, PC0, PC1, kp, vp, DOQK, DOPV, MID) do { \
            float rm_, ma_, mb_, mc_, md_, me_; f32x2 s0_, s1_; \
            bf16x8 k0_, k1_, k2_, k3_; \
            if (DOQK) { k1_ = KRD(kp, 1); k0_ = KRD(kp, 0); k3_ = KRD(kp, 3); k2_ = KRD(kp, 2); } \
            SB(); \
            APPLY_PEND(); \
              if (DOPV) o[0] = MFMA32(vf0, BC8(PP0), o[0]); RMAX1(SC); SB(); \
              if (DOPV) { o[1] = MFMA32(vf1, BC8(PP0), o[1]); vf1 = VRD(vp, 1, 1); vf0 = VRD(vp, 1, 0); } RMAX2(); SB(); \
              if (DOPV) o[2] = MFMA32(vf2, BC8(PP0), o[2]); SB(); \
              if (DOPV) { o[3] = MFMA32(vf3, BC8(PP0), o[3]); vf3 = VRD(vp, 1, 3); vf2 = VRD(vp, 1, 2); } SB(); \
            DECIDE(SC, PP1, DOPV); SB(); \
              if (DOQK) SN = MFMA32(k0_, qf[0], negm); EX1(SC, 0); EX1(SC, 1); PIN(SC); SB(); \
              if (DOPV) o[0] = MFMA32(vf0, BC8(PP1), o[0]); EX1(SC, 2); EX1(SC, 3); PC0[0] = cvtpk(SC[0], SC[1]); PIN(SC); PIN(PC0); SB(); \
              if (DOQK) { SN = MFMA32(k1_, qf[1], SN); k1_ = KRD(kp, 5); k0_ = KRD(kp, 4); } s0_ = PAIR(SC, 0) + PAIR(SC, 1); PIN2(s0_); EX1(SC, 4); EX1(SC, 5); PIN(SC); SB(); \
              if (DOPV) o[1] = MFMA32(vf1, BC8(PP1), o[1]); PC0[1] = cvtpk(SC[2], SC[3]); EX1(SC, 6); PC0[2] = cvtpk(SC[4], SC[5]); PIN(SC); PIN(PC0); SB(); \
              if (DOQK) SN = MFMA32(k2_, qf[2], SN); s0_ += PAIR(SC, 2); PIN2(s0_); EX1(SC, 7); EX1(SC, 8); PIN(SC); SB(); \
              if (DOPV) o[2] = MFMA32(vf2, BC8(PP1), o[2]); EX1(SC, 9); PC0[3] = cvtpk(SC[6], SC[7]); PIN(SC); PIN(PC0); SB(); \
              if (DOQK) { SN = MFMA32(k3_, qf[3], SN); k3_ = KRD(kp, 7); k2_ = KRD(kp, 6); } s1_ = PAIR(SC, 3) + PAIR(SC, 4); PIN2(s1_); EX1(SC, 10); EX1(SC, 11); PIN(SC); SB(); \
              if (DOPV) o[3] = MFMA32(vf3, BC8(PP1), o[3]); PC1[0] = cvtpk(SC[8], SC[9]); EX1(SC, 12); PC1[1] = cvtpk(SC[10], SC[11]); PIN(SC); PIN(PC1); SB(); \
              if (DOQK) SN = MFMA32(k0_, qf[4], SN); s1_ += PAIR(SC, 5); PIN2(s1_); EX1(SC, 13); EX1(SC, 14); PIN(SC); SB(); \
              if (DOQK) SN = MFMA32(k1_, qf[5], SN); EX1(SC, 15); PC1[2] = cvtpk(SC[12], SC[13]); PIN(SC); PIN(PC1); SB(); \
            MID; SB(); \
              if (DOQK) SN = MFMA32(k2_, qf[6], SN); s0_ += PAIR(SC, 6); s1_ += PAIR(SC, 7); PIN2(s0_); PIN2(s1_); PC1[3] = cvtpk(SC[14], SC[15]); PIN(PC1); SB(); \
              if (DOQK) SN = MFMA32(k3_, qf[7], SN); lsum += s0_; lsum += s1_; PIN2(lsum); SB(); \
        } while (0)
#define VPRE(a0, a1, vp) do { vf1 = VRDX(a0, a1, vp, 0, 1); vf0 = VRDX(a0, a1, vp, 0, 0); vf3 = VRDX(a0, a1, vp, 0, 3); vf2 = VRDX(a0, a1, vp, 0, 2); } while (0)
#define ABAR(Tn) do { LBAR(); } while (0)
        #pragma unroll 1
        for (int r = 0; r < 2; ++r) {
            {
                BASES();
#pragma unroll
                for (int ks = 0; ks < 8; ++ks) sA = MFMA32(KRD(0, ks), qf[ks], ks ? sA : (f32x16{}));
                float rm; { float a_ = MX3(sA[0], sA[1], sA[2]), b_ = MX3(sA[3], sA[4], sA[5]); a_ = MX3(a_, sA[6], sA[7]); b_ = MX3(b_, sA[8], sA[9]); a_ = MX3(a_, sA[10], sA[11]); b_ = MX3(b_, sA[12], sA[13]);
                    a_ = MX3(a_, sA[14], sA[15]); rm = swap32max(__builtin_fmaxf(a_, b_)); }
#pragma unroll
                for (int i = 0; i < 16; ++i) { sA[i] -= rm; negm[i] = -rm; }
                pend = false;
                SLOTM(sA, sB, pB0, pB1, pA0, pA1, 8192, 0, true, false, { if (r == 0) asm volatile("s_waitcnt vmcnt(0)" ::: "memory");
                    ABAR(16 * r + 1); VPRE(n0p, n1p, 0); });
                ADVANCE();
            }
#pragma unroll 1
            for (int t = 1; t < 16; ++t) {
                BASES();
                SLOTM(sB, sA, pA0, pA1, pB0, pB1, 0, 0, true, true, { VPRE(v0p, v1p, 8192); });
                SLOTM(sA, sB, pB0, pB1, pA0, pA1, 8192, 8192, true, true, { ABAR(16 * r + t + 1); VPRE(n0p, n1p, 0); });
                ADVANCE();
            }
            {
                BASES();
                SLOTM(sB, sA, pA0, pA1, pB0, pB1, 0, 0, false, true, { VPRE(v0p, v1p, 8192); });
                APPLY_PEND();
                o[0] = MFMA32(vf0, BC8(pB0), o[0]); o[1] = MFMA32(vf1, BC8(pB0), o[1]); vf1 = VRD(8192, 1, 1); vf0 = VRD(8192, 1, 0);
                o[2] = MFMA32(vf2, BC8(pB0), o[2]); o[3] = MFMA32(vf3, BC8(pB0), o[3]); vf3 = VRD(8192, 1, 3); vf2 = VRD(8192, 1, 2);
                o[0] = MFMA32(vf0, BC8(pB1), o[0]); o[1] = MFMA32(vf1, BC8(pB1), o[1]); o[2] = MFMA32(vf2, BC8(pB1), o[2]); o[3] = MFMA32(vf3, BC8(pB1), o[3]);
                if (r == 0) {
                    ETAG();
                    unsigned qa = lds0 + EX + 4096u * qb + lo16; asm volatile("" : "+v"(qa));
#pragma unroll
                    for (int s = 0; s < 8; ++s) qf[s] = ldsrd128((lds_cptr)(uintptr_t)qa + 16384 * (s >> 2) + 1024 * (s & 3));
                }
                const float inv = 1.0f / swap32sum(lsum[0] + lsum[1]);
                int ln = lane; asm volatile("" : "+v"(ln));
                const unsigned ovo = (unsigned)((ln >> 4) & 1) * 4096u + (unsigned)(ln & 15) * 16u + (unsigned)(ln >> 5) * 256u
                                   + (unsigned)((((g * 32 + h0 + r) * 8 + 2 * qb) * 4)) * 1024u;
                unsigned lo_ = lds0 + EX + 4096u * qb + (unsigned)((ln >> 4) & 1) * 16384u + (unsigned)(ln & 15) * 16u + (unsigned)(ln >> 5) * 256u; asm volatile("" : "+v"(lo_));
#pragma unroll
                for (int db = 0; db < 4; ++db)
#pragma unroll
                    for (int sg = 0; sg < 2; ++sg) { const bf16x8 pk_ = pack8s(o[db], sg, inv);
                        if (r == 0) *(LAS bf16x8*)(uintptr_t)(lo_ + 1024u * db + 512u * sg) = pk_; else { st16_sc1(BCU(pk_), ovo + 1024u * db + 512u * sg, srdO); pk2[2 * db + sg] = pk_; } }
#pragma unroll
                for (int i = 0; i < 4; ++i) o[i] = f32x16{};
                lsum = f32x2{0.f, 0.f};
            }
        }
                LBAR();
#pragma unroll
        for (int i = 0; i < 2; ++i)
#pragma unroll
            for (int c = 0; c < 9; ++c) oacc[i][c] = f32x4{};
    }
    {
        const int mg = w & 3, kq = w >> 2;
        int ln = lane; asm volatile("" : "+v"(ln));
        const unsigned ab0 = lds0 + (unsigned)(16 * ((ln >> 4) >> 1) + 4 * ((ln >> 4) & 1) + ((ln & 15) >> 2)) * 288u + 8u * (ln & 3);
#define TCHUNK(base, F) do { unsigned a0a = (base) + ab0; asm volatile("" : "+v"(a0a)); const lds_cptr kb_ = (lds_cptr)(uintptr_t)a0a; bf16x8 af_[18]; \
            _Pragma("unroll") for (int ks = 0; ks < 2; ++ks) _Pragma("unroll") for (int c = 0; c < 9; ++c) af_[9 * ks + c] = cat8(vtr(kb_ + 9216 * ks + 32 * c), vtr(kb_ + 9216 * ks + 2304 + 32 * c)); \
            SB(); \
            _Pragma("unroll") for (int ks = 0; ks < 2; ++ks) _Pragma("unroll") for (int c = 0; c < 9; ++c) { \
                oacc[0][c] = MFMA16(af_[9 * ks + c], (F)[2 * ks], oacc[0][c]); oacc[1][c] = MFMA16(af_[9 * ks + c], (F)[2 * ks + 1], oacc[1][c]); } \
            SB(); } while (0)
        if (kq == 0) {
            unsigned sw_ = lds0 + 73728u + 4096u * (unsigned)mg + (unsigned)((ln >> 4) & 1) * 2048u + (unsigned)(ln & 15) * 16u + (unsigned)(ln >> 5) * 256u;
            unsigned sr_ = lds0 + 73728u + 4096u * (unsigned)mg + (unsigned)ln * 16u;
            asm volatile("" : "+v"(sw_), "+v"(sr_));
#pragma unroll
            for (int hf = 0; hf < 2; ++hf) {
#pragma unroll
                for (int d = 0; d < 2; ++d)
#pragma unroll
                    for (int sg = 0; sg < 2; ++sg) *(LAS bf16x8*)(uintptr_t)(sw_ + 1024u * d + 512u * sg) = pk2[4 * hf + 2 * d + sg];
                asm volatile("" ::: "memory");
                bf16x8 f_[4];
#pragma unroll
                for (int d = 0; d < 2; ++d)
#pragma unroll
                    for (int m = 0; m < 2; ++m) f_[2 * d + m] = ldsrd128((lds_cptr)(uintptr_t)(sr_ + 2048u * m + 1024u * d));
                asm volatile("" ::: "memory");
                if (hf == 0) TCHUNK(WS1, f_); else TCHUNK(WS2, f_);
                if (hf == 0) {
                    asm volatile("s_waitcnt vmcnt(0)" ::: "memory");
                    if (ln == 0) st4_sc1(tag, WS_FO + (unsigned)((((g * 4 + hq) * 2 + 1) * 4 + mg) * 4 + c4) * 4u, srdF);
                }
            }
        }
        wait_tag(WS_FO + (unsigned)((((g * 4 + hq) * 2 + 1) * 4 + mg) * 4 + ((c4 + 1 + kq + (lane & 1)) & 3)) * 4u, srdF, tag);
                bf16x8 of[12];
#pragma unroll
        for (int i = 0; i < 6; ++i) {
            const int j2 = 4 + 6 * kq + i;
            const int hd = 8 * hq + 2 * ((c4 + (j2 >> 2)) & 3) + 1;
            const unsigned so = (unsigned)((((g * 32 + hd) * 8 + 2 * mg) * 4) + (j2 & 3)) * 1024u;
            LD16_SC1(of[2 * i], lo16, srdO, RFL(so)); LD16_SC1(of[2 * i + 1], lo16, srdO, RFL(so + 4096u));
        }
        asm volatile("s_waitcnt vmcnt(0)\n\ts_barrier" ::: "memory"); SB();
        asm volatile("" : "+v"(of[0]), "+v"(of[1]), "+v"(of[2]), "+v"(of[3]), "+v"(of[4]), "+v"(of[5]), "+v"(of[6]), "+v"(of[7]) :: "memory");
        asm volatile("" : "+v"(of[8]), "+v"(of[9]), "+v"(of[10]), "+v"(of[11]) :: "memory");
                TCHUNK(kq ? WCH : WS0, of); TCHUNK(kq ? 2u * WCH : WS3, of + 4); TCHUNK(kq ? 3u * WCH : 0u, of + 8);
        LBAR();
        const unsigned xw = (unsigned)w * 9216u + lo16, xr = (unsigned)(w ^ 4) * 9216u + lo16;
#pragma unroll
        for (int c = 0; c < 9; ++c) *(LAS f32x4*)(LW + xw + 1024 * c) = kq ? oacc[0][c] : oacc[1][c];
        LBAR();
        f32x4 fin[9];
#pragma unroll
        for (int c = 0; c < 9; ++c) { const f32x4 t4 = *(const LAS f32x4*)(LW + xr + 1024 * c); fin[c] = (kq ? oacc[1][c] : oacc[0][c]) + t4; }
        const int mb = 2 * mg + kq;
        const unsigned lo8 = (unsigned)lane * 8u;
                if (mg != hq) {
            const unsigned sso = (unsigned)((((g * 4 + c4) * 8 + mb) * 4 + hq) * 9) * 512u + lo8;
#pragma unroll
            for (int c = 0; c < 9; ++c) { const u32x2 v2 = {cvtpk(fin[c][0], fin[c][1]), cvtpk(fin[c][2], fin[c][3])}; st8_sc1(v2, sso + 512u * c, srdS); }
            asm volatile("s_waitcnt vmcnt(0)" ::: "memory");
            if (lane == 0) st4_sc1(tag, WS_FS + (unsigned)(((g * 4 + c4) * 8 + mb) * 4 + hq) * 4u, srdF);
        } else {
            const int ot = (hq + 1 + (lane % 3)) & 3;
            wait_tag(WS_FS + (unsigned)(((g * 4 + c4) * 8 + mb) * 4 + ot) * 4u, srdF, tag);
            if (hq == 0 && c4 == 0 && kq == 0 && lane == 0) st4_sc1(tag, WS_E + 64u * (unsigned)g, srdF);
            u32x2 sv[27];
#pragma unroll
            for (int m = 0; m < 3; ++m)
#pragma unroll
                for (int c = 0; c < 9; ++c) LD8_SC1(sv[9 * m + c], lo8, srdS, RFL((unsigned)((((g * 4 + c4) * 8 + mb) * 4 + ((hq + 1 + m) & 3)) * 9 + c) * 512u));
            asm volatile("s_waitcnt vmcnt(0)" : "+v"(sv[0]), "+v"(sv[1]), "+v"(sv[2]), "+v"(sv[3]), "+v"(sv[4]), "+v"(sv[5]), "+v"(sv[6]), "+v"(sv[7]), "+v"(sv[8]) :: "memory");
            asm volatile("" : "+v"(sv[9]), "+v"(sv[10]), "+v"(sv[11]), "+v"(sv[12]), "+v"(sv[13]), "+v"(sv[14]), "+v"(sv[15]), "+v"(sv[16]), "+v"(sv[17]) :: "memory");
            asm volatile("" : "+v"(sv[18]), "+v"(sv[19]), "+v"(sv[20]), "+v"(sv[21]), "+v"(sv[22]), "+v"(sv[23]), "+v"(sv[24]), "+v"(sv[25]), "+v"(sv[26]) :: "memory");
#pragma unroll
            for (int c = 0; c < 9; ++c) {
                f32x4 a = fin[c];
#pragma unroll
                for (int m = 0; m < 3; ++m) { const u32x2 v = sv[9 * m + c];
                    a[0] += __uint_as_float(v[0] << 16); a[1] += __uint_as_float(v[0] & 0xFFFF0000u); a[2] += __uint_as_float(v[1] << 16); a[3] += __uint_as_float(v[1] & 0xFFFF0000u); }
                const u32x2 ov = {cvtpk(a[0], a[1]), cvtpk(a[2], a[3])};
                *(u32x2*)(out + (size_t)(b * 128 + 16 * mb + (lane & 15)) * 1152 + 576 * p + 144 * c4 + 16 * c + 4 * g4) = ov;
            }
        }
    }
    }

extern "C" void kernel_launch(void* const* d_in, const int* in_sizes, int n_in, void* d_out, int out_size, void* d_ws, size_t ws_size, hipStream_t stream) {
    (void)hipFuncSetAttribute((const void*)fused_kernel, hipFuncAttributeMaxDynamicSharedMemorySize, LDS_BYTES);
    hipLaunchKernelGGL(fused_kernel, dim3(256), dim3(512), LDS_BYTES, stream,
                       (const char*)d_in[0], (const char*)d_in[1], (const char*)d_in[2], (const char*)d_in[3], (const char*)d_in[4],
                       (const char*)d_in[5], (const char*)d_in[6], (const char*)d_in[7], (const char*)d_in[8], (const char*)d_in[9],
                       (char*)d_ws, (unsigned short*)d_out);
}
```
